```python
import torch

def _softmax(x, axis=-1):
    return torch.softmax(x, dim=axis)
def _gelu(x):
    return torch.nn.functional.gelu(x, approximate="tanh")
def _silu(x):
    return torch.nn.functional.silu(x)
def _sigmoid(x):
    return torch.sigmoid(x)
def _layernorm(x, axis=-1, eps=1e-5):
    m = x.mean(dim=axis, keepdim=True)
    v = x.var(dim=axis, keepdim=True, unbiased=False)
    return (x - m) / torch.sqrt(v + eps)
def _scatter_add(tgt, idx, src, axis=0):
    return tgt.clone().index_add_(axis, idx, src)
def _segment_sum(src, seg, n):
    out = torch.zeros((n,) + src.shape[1:], dtype=src.dtype)
    return out.index_add_(0, seg, src)
def _pad(x, pw):
    flat = tuple(v for pair in reversed(pw) for v in pair)
    return torch.nn.functional.pad(x, flat)

kernel_name = 'standalone__mha_block_cached__near_ridge'

def setup_inputs():

    _w = (lambda a: (lambda t: t.to(torch.bfloat16) if t.is_floating_point() else t)(torch.as_tensor(a)))
    B, Sq, D = 8, 128, 640
    H, Sk, Dh = 32, 1024, 128
    return {
        "x": _w((lambda *s: torch.randn(*s, dtype=torch.bfloat16) * 0.8)(B, Sq, D)),
        "k_cache": _w((lambda *s: torch.randn(*s, dtype=torch.bfloat16) * 0.8)(B, H, Sk, Dh)),
        "v_cache": _w((lambda *s: torch.randn(*s, dtype=torch.bfloat16) * 0.05)(B, H, Sk, Dh)),
        "wq": _w((lambda *s: torch.randn(*s, dtype=torch.bfloat16) * 0.8)(D, H * Dh) * (D ** -0.5)),
        "wo": _w((lambda *s: torch.randn(*s, dtype=torch.bfloat16) * 0.05)(H * Dh, D) * ((H * Dh) ** -0.5)),
    }

def reference(x, k_cache, v_cache, wq, wo):
    B, Sq, D = x.shape
    H, Dh = 32, 128
    q = (lambda x, *d: x.permute(*d))((x @ wq).reshape(B, Sq, H, Dh), 0, 2, 1, 3)
    scores = torch.einsum("bhqd,bhkd->bhqk", q, k_cache) * (Dh ** -0.5)
    p = _softmax(scores, axis=-1)
    o = torch.einsum("bhqk,bhkd->bhqd", p, v_cache)
    return (lambda x, *d: x.permute(*d))(o, 0, 2, 1, 3).reshape(B, Sq, H * Dh) @ wo
```

```cpp
#include <hip/hip_runtime.h>
#include <hip/hip_bf16.h>

typedef short bf16x8 __attribute__((ext_vector_type(8)));
typedef short s16x4 __attribute__((ext_vector_type(4)));
typedef float f32x16 __attribute__((ext_vector_type(16)));
typedef float f32x4 __attribute__((ext_vector_type(4)));
typedef unsigned u32x4 __attribute__((ext_vector_type(4)));
typedef unsigned u32x2 __attribute__((ext_vector_type(2)));
typedef __bf16 bf16x2_t __attribute__((ext_vector_type(2)));
typedef float f32x2 __attribute__((ext_vector_type(2)));
#define LAS __attribute__((address_space(3)))
#define DI __device__ __forceinline__
#define MFMA(a, b, c) __builtin_amdgcn_mfma_f32_32x32x16_bf16((a), (b), (c), 0, 0, 0)

constexpr int SLOT = 32768;
constexpr int LDS_BYTES = 163840;
constexpr float QSCALE = 0.08838834764831845f * 1.4426950408889634f;
constexpr float THR = 8.0f;
constexpr size_t WS_O = 0;
constexpr size_t WS_P = 8u << 20;

DI unsigned cvtpk(float lo, float hi) { f32x2 v = {lo, hi}; bf16x2_t b = __builtin_convertvector(v, bf16x2_t); return __builtin_bit_cast(unsigned, b); }
DI bf16x8 pack8(const f32x16& x, int base) {
    u32x4 w = {cvtpk(x[base + 0], x[base + 1]), cvtpk(x[base + 2], x[base + 3]), cvtpk(x[base + 4], x[base + 5]), cvtpk(x[base + 6], x[base + 7])};
    return __builtin_bit_cast(bf16x8, w);
}
DI s16x4 trd(const LAS char* p) { return __builtin_bit_cast(s16x4, __builtin_amdgcn_ds_read_tr16_b64_v4i16((LAS s16x4*)p)); }
DI bf16x8 cat(s16x4 a, s16x4 b) { return __builtin_shufflevector(a, b, 0, 1, 2, 3, 4, 5, 6, 7); }
DI float swap32max(float v) { auto r = __builtin_amdgcn_permlane32_swap(__float_as_uint(v), __float_as_uint(v), false, false); return fmaxf(__uint_as_float(r[0]), __uint_as_float(r[1])); }
DI float swap32sum(float v) { auto r = __builtin_amdgcn_permlane32_swap(__float_as_uint(v), __float_as_uint(v), false, false); return __uint_as_float(r[0]) + __uint_as_float(r[1]); }
DI void dma_nt(__amdgpu_buffer_rsrc_t r, unsigned voff, unsigned soff, unsigned ldsaddr) {
    unsigned keep;
    asm volatile("s_mov_b32 %0, m0\n\ts_mov_b32 m0, %4\n\ts_nop 0\n\tbuffer_load_dwordx4 %1, %2, %3 offen nt lds\n\ts_mov_b32 m0, %0"
                 : "=&s"(keep) : "v"(voff), "s"(r), "s"(soff), "s"(ldsaddr) : "memory");
}
DI void dma_df(__amdgpu_buffer_rsrc_t r, unsigned voff, unsigned soff, unsigned ldsaddr) {
    unsigned keep;
    asm volatile("s_mov_b32 %0, m0\n\ts_mov_b32 m0, %4\n\ts_nop 0\n\tbuffer_load_dwordx4 %1, %2, %3 offen lds\n\ts_mov_b32 m0, %0"
                 : "=&s"(keep) : "v"(voff), "s"(r), "s"(soff), "s"(ldsaddr) : "memory");
}
#define ST(k)
#define WAIT_BAR(N) asm volatile("s_waitcnt vmcnt(" #N ") lgkmcnt(0)\n\ts_barrier" ::: "memory")
#define LBAR() asm volatile("s_waitcnt lgkmcnt(0)\n\ts_barrier" ::: "memory")
DI __amdgpu_buffer_rsrc_t mkrsrc(const void* p, unsigned bytes) { return __builtin_amdgcn_make_buffer_rsrc((void*)p, 0, bytes, 0x00020000); }

__global__ __launch_bounds__(512, 2) void k_attn(const char* __restrict__ x, const char* __restrict__ K, const char* __restrict__ V,
                                                 const char* __restrict__ wq, char* __restrict__ ws) {
    extern __shared__ __attribute__((aligned(16))) char lds_[];
    LAS char* lds = (LAS char*)lds_;
    const unsigned lds0 = (unsigned)(size_t)lds;
    const int wg = blockIdx.x, xg = wg & 7, idx = wg >> 3, b = idx >> 2, h = xg * 4 + (idx & 3);
    const int tid = threadIdx.x, l = tid & 63, w = __builtin_amdgcn_readfirstlane(tid >> 6), g = w & 3, c = w >> 2;
    const int r = l & 31, hh = l >> 5;
    const size_t bh = (size_t)(b * 32 + h);
    const __amdgpu_buffer_rsrc_t rK = mkrsrc(K + bh * 262144, 262144), rV = mkrsrc(V + bh * 262144, 262144);
    const __amdgpu_buffer_rsrc_t rX = mkrsrc(x + (size_t)b * 163840, 163840), rW = mkrsrc(wq + h * 256, 640 * 8192 - h * 256);
    const unsigned lrow = (l >> 2) & 7, lsub = (l >> 5) * 64, l4 = (l >> 4) & 1, l3 = l & 3;

    ST(0);
    u32x4 park[4][8];
    if (c == 1) {
        const unsigned vx0 = lrow * 1280 + lsub + ((l3 ^ (l4)) * 16), vx1 = lrow * 1280 + lsub + ((l3 ^ (2 + l4)) * 16);
        const unsigned vw0 = lrow * 8192 + lsub + ((l3 ^ (l4)) * 16), vw1 = lrow * 8192 + lsub + ((l3 ^ (2 + l4)) * 16);
#pragma unroll
        for (int j = 0; j < 5; ++j) {
#pragma unroll
            for (int i = 0; i < 4; ++i) dma_df(rX, (i & 1) ? vx1 : vx0, (8 * (4 * g + i)) * 1280 + j * 128, lds0 + j * SLOT + (4 * g + i) * 1024);
#pragma unroll
            for (int i = 0; i < 4; ++i) dma_df(rW, (i >> 1) ? vw1 : vw0, (64 * j + 8 * (2 * g + (i >> 1))) * 8192 + (i & 1) * 128, lds0 + j * SLOT + 16384 + (4 * g + i) * 1024);
        }
    } else {
        const unsigned v0 = lrow * 256 + lsub + ((l3 ^ (l4)) * 16), v1 = lrow * 256 + lsub + ((l3 ^ (2 + l4)) * 16);
#pragma unroll
        for (int t = 0; t < 4; ++t) {
#pragma unroll
            for (int i = 0; i < 4; ++i) park[t][i] = __builtin_amdgcn_raw_buffer_load_b128(rK, (i >> 1) ? v1 : v0, t * 16384 + 8 * (2 * g + (i >> 1)) * 256 + (i & 1) * 128, 2);
#pragma unroll
            for (int i = 0; i < 4; ++i) park[t][4 + i] = __builtin_amdgcn_raw_buffer_load_b128(rV, (i >> 1) ? v1 : v0, t * 16384 + 8 * (2 * g + (i >> 1)) * 256 + (i & 1) * 128, 2);
        }
    }
    f32x16 qa0 = {}, qa1 = {};
    {
        const int row = 32 * g + r;
        const unsigned xb0 = 1024 * (row >> 3) + 64 * (row & 7) + 16 * ((hh) ^ ((row >> 2) & 3));
        const unsigned xb1 = 1024 * (row >> 3) + 64 * (row & 7) + 16 * ((2 + hh) ^ ((row >> 2) & 3));
        const int blk = (l >> 4) & 1, q = (l & 15) >> 2, p = l & 3, pp = ((p & 1) << 1) | (p >> 1);
        const unsigned wb0 = 16384 + 2048 * hh + 1024 * c + 64 * q + 16 * ((2 * blk + (pp >> 1)) ^ (2 * hh)) + 8 * (pp & 1);
        const unsigned wb1 = 16384 + 2048 * hh + 1024 * c + 256 + 64 * q + 16 * ((2 * blk + (pp >> 1)) ^ (2 * hh + 1)) + 8 * (pp & 1);
        const unsigned vx0 = lrow * 1280 + lsub + ((l3 ^ (l4)) * 16), vx1 = lrow * 1280 + lsub + ((l3 ^ (2 + l4)) * 16);
        const unsigned vw0 = lrow * 8192 + lsub + ((l3 ^ (l4)) * 16), vw1 = lrow * 8192 + lsub + ((l3 ^ (2 + l4)) * 16);
#pragma unroll
        for (int j = 0; j < 10; ++j) {
            if (c == 1) {
                if (j == 0) asm volatile("s_waitcnt vmcnt(32)" ::: "memory");
                else if (j <= 6) asm volatile("s_waitcnt vmcnt(24)" ::: "memory");
                else if (j == 7) asm volatile("s_waitcnt vmcnt(16)" ::: "memory");
                else if (j == 8) asm volatile("s_waitcnt vmcnt(8)" ::: "memory");
                else asm volatile("s_waitcnt vmcnt(0)" ::: "memory");
            }
            LBAR();
            ST(1 + j);
            if (c == 1) {
                if (j >= 1 && j + 4 <= 9) {
                    const int jj = j + 4, sl = (j - 1) % 5;
#pragma unroll
                    for (int i = 0; i < 4; ++i) dma_df(rX, (i & 1) ? vx1 : vx0, (8 * (4 * g + i)) * 1280 + jj * 128, lds0 + sl * SLOT + (4 * g + i) * 1024);
#pragma unroll
                    for (int i = 0; i < 4; ++i) dma_df(rW, (i >> 1) ? vw1 : vw0, (64 * jj + 8 * (2 * g + (i >> 1))) * 8192 + (i & 1) * 128, lds0 + sl * SLOT + 16384 + (4 * g + i) * 1024);
                }
            } else if (j >= 6) {
                const int t = j - 6;
#pragma unroll
                for (int i = 0; i < 4; ++i) {
                    *(LAS u32x4*)(lds + t * SLOT + (4 * g + i) * 1024 + l * 16) = park[t][i];
                    *(LAS u32x4*)(lds + t * SLOT + 16384 + (4 * g + i) * 1024 + l * 16) = park[t][4 + i];
                }
            }
            const LAS char* sb = lds + (j % 5) * SLOT;
#pragma unroll
            for (int ks = 0; ks < 4; ++ks) {
                const bf16x8 xf = *(const LAS bf16x8*)(sb + ((ks & 1) ? xb1 : xb0) + 512 * (ks >> 1));
                const bf16x8 a0 = cat(trd(sb + wb0 + 4096 * ks), trd(sb + wb1 + 4096 * ks));
                const bf16x8 a1 = cat(trd(sb + wb0 + 4096 * ks + 512), trd(sb + wb1 + 4096 * ks + 512));
                qa0 = MFMA(a0, xf, qa0);
                qa1 = MFMA(a1, xf, qa1);
            }
        }
    }
    bf16x8 qf[8];
    {
#pragma unroll
        for (int i = 0; i < 16; ++i) { qa0[i] *= QSCALE; qa1[i] *= QSCALE; }
        bf16x8 m0 = pack8(qa0, 0), m1 = pack8(qa0, 8), m2 = pack8(qa1, 0), m3 = pack8(qa1, 8);
        LBAR();
        LAS char* qs = lds + 4 * SLOT + g * 8192 + l * 16;
        *(LAS bf16x8*)(qs + (4 * c + 0) * 1024) = m0; *(LAS bf16x8*)(qs + (4 * c + 1) * 1024) = m1;
        *(LAS bf16x8*)(qs + (4 * c + 2) * 1024) = m2; *(LAS bf16x8*)(qs + (4 * c + 3) * 1024) = m3;
        LBAR();
#pragma unroll
        for (int i = 0; i < 8; ++i) qf[i] = *(const LAS bf16x8*)(qs + i * 1024);
    }
    f32x16 o[4] = {}; f32x16 negm = {}; float m = 0.f, lsum = 0.f;
    {
        const unsigned vd = (8 * w + lrow) * 256 + lsub + ((l3 ^ ((2 * w + l4) & 3)) * 16);
        const int krow = 32 * c + r;
        const unsigned kb0 = 2048 * (krow >> 3) + 64 * (krow & 7) + 16 * ((hh) ^ ((krow >> 2) & 3));
        const unsigned kb1 = 2048 * (krow >> 3) + 64 * (krow & 7) + 16 * ((2 + hh) ^ ((krow >> 2) & 3));
        const int blk = (l >> 4) & 1, q = (l & 15) >> 2, p = l & 3;
        const unsigned vb0 = 16384 + 8192 * c + 64 * (4 * hh + q) + 16 * ((2 * blk + (p >> 1)) ^ (hh)) + 8 * (p & 1);
        const unsigned vb1 = 16384 + 8192 * c + 2048 + 64 * (4 * hh + q) + 16 * ((2 * blk + (p >> 1)) ^ (2 + hh)) + 8 * (p & 1);
        int slot = 0;
        for (int t = 0; t < 16; ++t) {
            if (t <= 12) WAIT_BAR(12); else if (t == 13) WAIT_BAR(8); else if (t == 14) WAIT_BAR(4); else WAIT_BAR(0);
            ST(11 + t);
            if (t + 4 < 16) {
                const int sl = slot == 0 ? 4 : slot - 1; const unsigned d = lds0 + sl * SLOT + 2 * w * 1024, so = (t + 4) * 16384;
                dma_nt(rK, vd, so, d); dma_nt(rK, vd, so + 128, d + 1024); dma_nt(rV, vd, so, d + 16384); dma_nt(rV, vd, so + 128, d + 16384 + 1024);
            }
            const LAS char* sb = lds + slot * SLOT;
            f32x16 s = negm;
#pragma unroll
            for (int ks = 0; ks < 8; ++ks) {
                const bf16x8 kf = *(const LAS bf16x8*)(sb + ((ks & 1) ? kb1 : kb0) + 512 * (ks >> 1));
                s = MFMA(kf, qf[ks], s);
            }
            bf16x8 vf[4][2];
#pragma unroll
            for (int ks = 0; ks < 2; ++ks)
#pragma unroll
                for (int db = 0; db < 4; ++db) vf[db][ks] = cat(trd(sb + vb0 + 4096 * ks + 512 * db), trd(sb + vb1 + 4096 * ks + 512 * db));
            float mx = fmaxf(fmaxf(s[0], s[1]), s[2]);
#pragma unroll
            for (int i = 3; i < 15; i += 2) mx = fmaxf(fmaxf(mx, s[i]), s[i + 1]);
            mx = fmaxf(mx, s[15]);
            mx = swap32max(mx);
            if (t == 0 || __any(mx > THR)) {
                const float delta = (t == 0) ? mx : fmaxf(mx, 0.f);
                const float f = __builtin_amdgcn_exp2f(-fmaxf(delta, 0.f));
                m += delta; lsum *= f;
#pragma unroll
                for (int i = 0; i < 16; ++i) { s[i] -= delta; negm[i] = -m; }
#pragma unroll
                for (int db = 0; db < 4; ++db)
#pragma unroll
                    for (int i = 0; i < 16; ++i) o[db][i] *= f;
            }
            float ps = 0.f;
#pragma unroll
            for (int i = 0; i < 16; ++i) { s[i] = __builtin_amdgcn_exp2f(s[i]); ps += s[i]; }
            lsum += ps;
            const bf16x8 p0 = pack8(s, 0), p1 = pack8(s, 8);
#pragma unroll
            for (int db = 0; db < 4; ++db) o[db] = MFMA(vf[db][0], p0, o[db]);
#pragma unroll
            for (int db = 0; db < 4; ++db) o[db] = MFMA(vf[db][1], p1, o[db]);
            slot = slot == 4 ? 0 : slot + 1;
        }
    }
    {
        ST(27);
        lsum = swap32sum(lsum);
        LAS char* mine = lds + SLOT + w * 8192 + l * 16;
        LAS char* other = lds + SLOT + (w ^ 4) * 8192 + l * 16;
        LAS f32x2* ml = (LAS f32x2*)(lds + 3 * SLOT);
        if (c == 0) {
#pragma unroll
            for (int i = 0; i < 8; ++i) *(LAS f32x4*)(mine + i * 1024) = (f32x4){o[2 + (i >> 2)][4 * (i & 3)], o[2 + (i >> 2)][4 * (i & 3) + 1], o[2 + (i >> 2)][4 * (i & 3) + 2], o[2 + (i >> 2)][4 * (i & 3) + 3]};
        } else {
#pragma unroll
            for (int i = 0; i < 8; ++i) *(LAS f32x4*)(mine + i * 1024) = (f32x4){o[(i >> 2)][4 * (i & 3)], o[(i >> 2)][4 * (i & 3) + 1], o[(i >> 2)][4 * (i & 3) + 2], o[(i >> 2)][4 * (i & 3) + 3]};
            o[0] = o[2]; o[1] = o[3];
        }
        ml[w * 64 + l] = (f32x2){m, lsum};
        LBAR();
        const f32x2 om = ml[(w ^ 4) * 64 + l];
        const float M = fmaxf(m, om[0]), a = __builtin_amdgcn_exp2f(m - M), bb = __builtin_amdgcn_exp2f(om[0] - M);
        const float inv = 1.0f / (a * lsum + bb * om[1]), sa = a * inv, sbb = bb * inv;
#pragma unroll
        for (int i = 0; i < 8; ++i) {
            const f32x4 v = *(const LAS f32x4*)(other + i * 1024);
#pragma unroll
            for (int e = 0; e < 4; ++e) o[i >> 2][4 * (i & 3) + e] = o[i >> 2][4 * (i & 3) + e] * sa + v[e] * sbb;
        }
        char* dst = ws + WS_O + ((bh * 4 + g) * 8 + 4 * c) * 1024 + l * 16;
        *(bf16x8*)(dst) = pack8(o[0], 0); *(bf16x8*)(dst + 1024) = pack8(o[0], 8);
        *(bf16x8*)(dst + 2048) = pack8(o[1], 0); *(bf16x8*)(dst + 3072) = pack8(o[1], 8);
        ST(28);
    }
}

__global__ __launch_bounds__(512, 2) void k_oproj(const char* __restrict__ wo, char* __restrict__ ws) {
    extern __shared__ __attribute__((aligned(16))) char lds_[];
    LAS char* lds = (LAS char*)lds_;
    const unsigned lds0 = (unsigned)(size_t)lds;
    const int wg = blockIdx.x, xg = wg & 7, idx = wg >> 3, b = idx >> 2, nq = idx & 3;
    const int tid = threadIdx.x, l = tid & 63, w = __builtin_amdgcn_readfirstlane(tid >> 6), g = w & 3, c = w >> 2;
    const int hh = l >> 5;
    const __amdgpu_buffer_rsrc_t rW = mkrsrc(wo + (size_t)xg * 512 * 1280 + nq * 320, 512 * 1280 - nq * 320);
    const unsigned lrow = (l >> 2) & 7, l4 = (l >> 4) & 1, l3 = l & 3;
#pragma unroll
    for (int pi = 0; pi < 5; ++pi) {
        const unsigned u = 2 * pi + (l >> 5), rg = u >= 5 ? 1 : 0, cb = u - 5 * rg;
        const unsigned vo = (8 * rg + lrow) * 1280 + cb * 64 + ((l3 ^ ((2 * rg + l4) & 3)) * 16);
#pragma unroll
        for (int pe = 0; pe < 4; ++pe) dma_df(rW, vo, (4 * w + pe) * 16 * 1280, lds0 + (4 * w + pe) * 5120 + pi * 1024);
    }
    bf16x8 of[4][4];
#pragma unroll
    for (int j = 0; j < 4; ++j)
#pragma unroll
        for (int i = 0; i < 4; ++i) of[j][i] = *(const bf16x8*)(ws + WS_O + ((((size_t)(b * 32 + 4 * xg + j)) * 4 + g) * 8 + 4 * c + i) * 1024 + l * 16);
    WAIT_BAR(0);
    const int blk = (l >> 4) & 1, q = (l & 15) >> 2, p = l & 3;
    const unsigned wb0 = 20480 * c + 64 * (4 * hh + q) + 16 * ((2 * blk + (p >> 1)) ^ (hh)) + 8 * (p & 1);
    const unsigned wb1 = 20480 * c + 2560 + 64 * (4 * hh + q) + 16 * ((2 * blk + (p >> 1)) ^ (2 + hh)) + 8 * (p & 1);
    f32x16 acc[5] = {};
#pragma unroll
    for (int j = 0; j < 4; ++j) {
        const LAS char* sb = lds + j * 40960;
#pragma unroll
        for (int i = 0; i < 4; ++i)
#pragma unroll
            for (int nb = 0; nb < 5; ++nb) {
                const bf16x8 a = cat(trd(sb + wb0 + 5120 * i + 512 * nb), trd(sb + wb1 + 5120 * i + 512 * nb));
                acc[nb] = MFMA(a, of[j][i], acc[nb]);
            }
    }
    LBAR();
    LAS char* mine = lds + w * 10240 + l * 16;
    LAS char* other = lds + (w ^ 4) * 10240 + l * 16;
    char* dst = ws + WS_P + ((((size_t)(b * 4 + nq) * 8 + xg) * 4 + g) * 20 + 10 * c) * 1024 + l * 16;
#define QUAD(u) (f32x4){acc[(u) >> 2][4 * ((u) & 3)], acc[(u) >> 2][4 * ((u) & 3) + 1], acc[(u) >> 2][4 * ((u) & 3) + 2], acc[(u) >> 2][4 * ((u) & 3) + 3]}
    if (c == 0) {
#pragma unroll
        for (int i = 0; i < 10; ++i) *(LAS f32x4*)(mine + i * 1024) = QUAD(10 + i);
        LBAR();
#pragma unroll
        for (int i = 0; i < 10; ++i) *(f32x4*)(dst + i * 1024) = QUAD(i) + *(const LAS f32x4*)(other + i * 1024);
    } else {
#pragma unroll
        for (int i = 0; i < 10; ++i) *(LAS f32x4*)(mine + i * 1024) = QUAD(i);
        LBAR();
#pragma unroll
        for (int i = 0; i < 10; ++i) *(f32x4*)(dst + i * 1024) = QUAD(10 + i) + *(const LAS f32x4*)(other + i * 1024);
    }
}

__global__ __launch_bounds__(256) void k_reduce(const char* __restrict__ ws, char* __restrict__ out) {
    const int it = blockIdx.x * 256 + threadIdx.x, l = it & 63, rest = it >> 6, u = rest % 20, g = (rest / 20) & 3, bn = rest / 80, nq = bn & 3, b = bn >> 2;
    const char* src = ws + WS_P + (((size_t)bn * 8 * 4 + g) * 20 + u) * 1024 + l * 16;
    f32x4 s = *(const f32x4*)src;
#pragma unroll
    for (int xg = 1; xg < 8; ++xg) s += *(const f32x4*)(src + (size_t)xg * 81920);
    const int n = 160 * nq + 32 * (u >> 2) + 8 * (u & 3) + 4 * (l >> 5), qrow = 32 * g + (l & 31);
    *(u32x2*)(out + ((size_t)(b * 128 + qrow) * 640 + n) * 2) = (u32x2){cvtpk(s[0], s[1]), cvtpk(s[2], s[3])};
}

extern "C" void kernel_launch(void* const* d_in, const int* in_sizes, int n_in, void* d_out, int out_size, void* d_ws, size_t ws_size, hipStream_t stream) {
    (void)hipFuncSetAttribute((const void*)k_attn, hipFuncAttributeMaxDynamicSharedMemorySize, LDS_BYTES);
    (void)hipFuncSetAttribute((const void*)k_oproj, hipFuncAttributeMaxDynamicSharedMemorySize, LDS_BYTES);
    k_attn<<<256, 512, LDS_BYTES, stream>>>((const char*)d_in[0], (const char*)d_in[1], (const char*)d_in[2], (const char*)d_in[3], (char*)d_ws);
    k_oproj<<<256, 512, LDS_BYTES, stream>>>((const char*)d_in[4], (char*)d_ws);
    k_reduce<<<640, 256, 0, stream>>>((const char*)d_ws, (char*)d_out);
}
```

```cpp
#include <hip/hip_runtime.h>
#include <hip/hip_bf16.h>

typedef short bf16x8 __attribute__((ext_vector_type(8)));
typedef short s16x4 __attribute__((ext_vector_type(4)));
typedef float f32x16 __attribute__((ext_vector_type(16)));
typedef float f32x4 __attribute__((ext_vector_type(4)));
typedef unsigned u32x4 __attribute__((ext_vector_type(4)));
typedef unsigned u32x2 __attribute__((ext_vector_type(2)));
typedef __bf16 bf16x2_t __attribute__((ext_vector_type(2)));
typedef float f32x2 __attribute__((ext_vector_type(2)));
#define LAS __attribute__((address_space(3)))
#define DI __device__ __forceinline__
#define MFMA(a, b, c) __builtin_amdgcn_mfma_f32_32x32x16_bf16((a), (b), (c), 0, 0, 0)

constexpr int SLOT = 32768;
constexpr int LDS_BYTES = 163840;
constexpr float QSCALE = 0.08838834764831845f * 1.4426950408889634f;
constexpr float PSUM_MAX = 4096.0f;
constexpr unsigned WS_O = 0;
constexpr unsigned WS_P = 8u << 20;
constexpr unsigned WS_M = 20u << 20;
constexpr unsigned WS_R = WS_M + 32768;

DI unsigned cvtpk(float lo, float hi) { f32x2 v = {lo, hi}; bf16x2_t b = __builtin_convertvector(v, bf16x2_t); return __builtin_bit_cast(unsigned, b); }
DI bf16x8 pack8(const f32x16& x, int base) {
    u32x4 w = {cvtpk(x[base + 0], x[base + 1]), cvtpk(x[base + 2], x[base + 3]), cvtpk(x[base + 4], x[base + 5]), cvtpk(x[base + 6], x[base + 7])};
    return __builtin_bit_cast(bf16x8, w);
}
DI s16x4 trd(const LAS char* p) { return __builtin_bit_cast(s16x4, __builtin_amdgcn_ds_read_tr16_b64_v4i16((LAS s16x4*)p)); }
DI bf16x8 cat(s16x4 a, s16x4 b) { return __builtin_shufflevector(a, b, 0, 1, 2, 3, 4, 5, 6, 7); }
DI float swap32max(float v) { auto r = __builtin_amdgcn_permlane32_swap(__float_as_uint(v), __float_as_uint(v), false, false); return fmaxf(__uint_as_float(r[0]), __uint_as_float(r[1])); }
DI float swap32sum(float v) { auto r = __builtin_amdgcn_permlane32_swap(__float_as_uint(v), __float_as_uint(v), false, false); return __uint_as_float(r[0]) + __uint_as_float(r[1]); }
DI float max16(const f32x16& s) {
    float a = fmaxf(fmaxf(s[0], s[1]), s[2]), b = fmaxf(fmaxf(s[3], s[4]), s[5]);
    a = fmaxf(fmaxf(a, s[6]), s[7]); b = fmaxf(fmaxf(b, s[8]), s[9]); a = fmaxf(fmaxf(a, s[10]), s[11]); b = fmaxf(fmaxf(b, s[12]), s[13]);
    return fmaxf(fmaxf(fmaxf(a, s[14]), s[15]), b);
}
DI void dma_nt(__amdgpu_buffer_rsrc_t r, unsigned voff, unsigned soff, unsigned ldsaddr) {
    asm volatile("s_mov_b32 m0, %3\n\ts_nop 0\n\tbuffer_load_dwordx4 %0, %1, %2 offen nt lds" :: "v"(voff), "s"(r), "s"(soff), "s"(ldsaddr) : "memory");
}
DI void dma_df(__amdgpu_buffer_rsrc_t r, unsigned voff, unsigned soff, unsigned ldsaddr) {
    asm volatile("s_mov_b32 m0, %3\n\ts_nop 0\n\tbuffer_load_dwordx4 %0, %1, %2 offen lds" :: "v"(voff), "s"(r), "s"(soff), "s"(ldsaddr) : "memory");
}
DI u32x4 ld_df(__amdgpu_buffer_rsrc_t r, unsigned voff, unsigned soff) {
    u32x4 v; asm volatile("buffer_load_dwordx4 %0, %1, %2, %3 offen" : "=v"(v) : "v"(voff), "s"(r), "s"(soff) : "memory"); return v;
}
DI u32x4 ld_nt(__amdgpu_buffer_rsrc_t r, unsigned voff, unsigned soff) {
    u32x4 v; asm volatile("buffer_load_dwordx4 %0, %1, %2, %3 offen nt" : "=v"(v) : "v"(voff), "s"(r), "s"(soff) : "memory"); return v;
}
#define ST(k)
#define STC(k)
#define WAIT_BAR(N) asm volatile("s_waitcnt vmcnt(" #N ") lgkmcnt(0)\n\ts_barrier" ::: "memory")
#define LBAR() asm volatile("s_waitcnt lgkmcnt(0)\n\ts_barrier" ::: "memory")
#define PIN4(a) asm volatile("" : "+v"(a[0]), "+v"(a[1]), "+v"(a[2]), "+v"(a[3]))
#define SB() __builtin_amdgcn_sched_barrier(0)
#define PIN(x) asm volatile("" : "+v"(x))
#define EX(x) __builtin_amdgcn_exp2f(x)
#define AMM(D, A, B) asm volatile("v_mfma_f32_32x32x16_bf16 %0, %1, %2, %0" : "+v"(D) : "v"(A), "v"(B))
#define AMM0(D, A, B, C0) asm volatile("v_mfma_f32_32x32x16_bf16 %0, %1, %2, %3" : "=&v"(D) : "v"(A), "v"(B), "v"(C0))
DI __amdgpu_buffer_rsrc_t mkrsrc(const void* p, unsigned bytes) { return __builtin_amdgcn_make_buffer_rsrc((void*)p, 0, bytes, 0x00020000); }

constexpr int K0_OFF = 131072, CTL_OFF = 147456;
constexpr int PL_OFF = CTL_OFF, PC_OFF = CTL_OFF + 64, SY_OFF = CTL_OFF + 128, TOK_OFF = CTL_OFF + 132;
DI void lds_post(unsigned addr, unsigned v) { asm volatile("s_mov_b64 exec, 1\n\tds_write_b32 %0, %1\n\ts_mov_b64 exec, -1" :: "v"(addr), "v"(v) : "memory"); }
DI u32x4 lds_peek4(const LAS char* p) { return *(const volatile LAS u32x4*)p; }
DI unsigned lds_peek(const LAS char* p) { return __builtin_amdgcn_readfirstlane(*(const volatile LAS unsigned*)p); }
DI unsigned min4(u32x4 v) { return __builtin_amdgcn_readfirstlane(min(min(v[0], v[1]), min(v[2], v[3]))); }
typedef __attribute__((address_space(1))) unsigned gu32;
#define RLX_AGENT __ATOMIC_RELAXED, __HIP_MEMORY_SCOPE_AGENT
constexpr int SPIN_CAP = 1 << 18;

DI void handoff(char* ws, int me, int pw, bool act, int myslot, int pslot, unsigned tok, unsigned pre, unsigned syaddr, unsigned syval) {
    gu32* pm = (gu32*)(ws + WS_M + pw * 128); gu32* pr = (gu32*)(ws + WS_R + pw * 128) + myslot; gu32* mr = (gu32*)(ws + WS_R + me * 128) + pslot;
    unsigned last = pre;
    if (act) __hip_atomic_store(pr, pre, RLX_AGENT);
    for (int sp = 0; sp < SPIN_CAP; ++sp) {
        unsigned rj = tok;
        if (act) rj = __hip_atomic_load(mr, RLX_AGENT);
        if (__all(rj == tok)) break;
        if ((sp & 3) == 3 && act) { const unsigned mj = __hip_atomic_load(pm, RLX_AGENT); if (mj != last) { __hip_atomic_store(pr, mj, RLX_AGENT); last = mj; } }
        __builtin_amdgcn_s_sleep(1);
    }
    asm volatile("" ::: "memory");
    lds_post(syaddr, syval);
    if (act) { const unsigned mj = __hip_atomic_load(pm, RLX_AGENT); if (mj != last) __hip_atomic_store(pr, mj, RLX_AGENT); }
}

__global__ __launch_bounds__(512, 2) void k_fused(const char* __restrict__ x, const char* __restrict__ K, const char* __restrict__ V,
                                                  const char* __restrict__ wq, const char* __restrict__ wo, char* __restrict__ out, char* __restrict__ ws) {
    extern __shared__ __attribute__((aligned(16))) char lds_[];
    LAS char* lds = (LAS char*)lds_;
    const unsigned lds0 = (unsigned)(size_t)lds;
    const int wg = blockIdx.x, xg = wg & 7, idx = wg >> 3, b = idx >> 2, hl = idx & 3, h = xg * 4 + hl;
    const int tid = threadIdx.x, l = tid & 63, w = __builtin_amdgcn_readfirstlane(tid >> 6), g = w & 3, c = w >> 2;
    const int r = l & 31, hh = l >> 5;
    const size_t bh = (size_t)(b * 32 + h);
    const __amdgpu_buffer_rsrc_t rWS = mkrsrc(ws, 20u << 20);
    ST(0);
    if (tid < 40) *(LAS unsigned*)(lds + CTL_OFF + tid * 4) = 0u;
#define AWAITP(have, off, n, SL) do { int sp_ = 0; while ((have = min4(lds_peek4(lds + (off)))) <= (unsigned)(n) && ++sp_ < SPIN_CAP) __builtin_amdgcn_s_sleep(SL); asm volatile("" ::: "memory"); } while (0)
#define AWAIT1(off, n) do { int sp_ = 0; while (lds_peek(lds + (off)) < (unsigned)(n) && ++sp_ < SPIN_CAP) __builtin_amdgcn_s_sleep(1); asm volatile("" ::: "memory"); } while (0)
#define VMW(N) asm volatile("s_waitcnt vmcnt(" #N ")" ::: "memory")
#define REDUCE(un) do { const int ui_ = 5 * xg + (un), ug_ = ui_ / 10, uu_ = ui_ % 10; u32x4 pv_[8]; \
        _Pragma("unroll") for (int j = 0; j < 8; ++j) pv_[j] = __builtin_amdgcn_raw_buffer_load_b128(rWS, l * 16, WS_P + ((idx * 8 + j) * 40 + ui_) * 1024, 16); \
        float a_[8] = {}; \
        _Pragma("unroll") for (int j = 0; j < 8; ++j) _Pragma("unroll") for (int e = 0; e < 4; ++e) { a_[2 * e] += __uint_as_float(pv_[j][e] << 16); a_[2 * e + 1] += __uint_as_float(pv_[j][e] & 0xffff0000u); } \
        char* o_ = out + ((size_t)(b * 128 + 32 * ug_ + r) * 640 + 160 * hl + 16 * uu_ + 4 * hh) * 2; \
        *(u32x2*)o_ = (u32x2){cvtpk(a_[0], a_[1]), cvtpk(a_[2], a_[3])}; *(u32x2*)(o_ + 16) = (u32x2){cvtpk(a_[4], a_[5]), cvtpk(a_[6], a_[7])}; } while (0)
    if (c == 1) {
        const __amdgpu_buffer_rsrc_t rK = mkrsrc(K + bh * 262144, 262144), rV = mkrsrc(V + bh * 262144, 262144);
        const __amdgpu_buffer_rsrc_t rX = mkrsrc(x + (size_t)b * 163840, 163840), rW = mkrsrc(wq + h * 256, 640 * 8192 - h * 256);
        const __amdgpu_buffer_rsrc_t rO = mkrsrc(wo + (size_t)xg * 512 * 1280 + hl * 320, 512 * 1280 - hl * 320);
#define GRPM 0
        const bool grpA = GRPM == 0 ? (((b ^ h) & 1) != 0) : (GRPM == 1);
        const unsigned lrow = (l >> 2) & 7, lsub = (l >> 5) * 64, l4 = (l >> 4) & 1, l3 = l & 3, rq = (l >> 2) & 3, csub = (l >> 4) * 64;
        const unsigned vk = (4 * g + rq) * 256 + csub + ((l3 ^ g) * 16), vw = (4 * g + rq) * 8192 + csub + ((l3 ^ g) * 16);
        const unsigned vx0 = (32 * g + lrow) * 1280 + lsub + ((l3 ^ l4) * 16), vx1 = (32 * g + 8 + lrow) * 1280 + lsub + ((l3 ^ (2 + l4)) * 16);
        const unsigned dk = lds0 + g * 1024, dx = lds0 + 4 * g * 1024;
#define XHALF(sl) do { const unsigned d_ = dx + (sl) * SLOT, sx_ = jr * 128; \
        dma_df(rX, vx0, sx_, d_); dma_df(rX, vx1, sx_, d_ + 1024); dma_df(rX, vx0, sx_ + 16 * 1280, d_ + 2048); dma_df(rX, vx1, sx_ + 16 * 1280, d_ + 3072); } while (0)
#define WHALF(sl) do { const unsigned e_ = dk + (sl) * SLOT + 16384, sq_ = jr * (64 * 8192); \
        dma_nt(rW, vw, sq_, e_); dma_nt(rW, vw, sq_ + 16 * 8192, e_ + 4096); dma_nt(rW, vw, sq_ + 32 * 8192, e_ + 8192); dma_nt(rW, vw, sq_ + 48 * 8192, e_ + 12288); } while (0)
#define CHUNK(sl) do { XHALF(sl); WHALF(sl); jr = jr == 9 ? 0 : jr + 1; } while (0)
#define PARK4(P, R, off) do { P[0] = ld_nt(R, vk, (off)); P[1] = ld_nt(R, vk, (off) + 4096); P[2] = ld_nt(R, vk, (off) + 8192); P[3] = ld_nt(R, vk, (off) + 12288); } while (0)
#define UNPARK4(P, off) do { PIN4(P); LAS char* d_ = lds + (off) + g * 1024 + l * 16; *(LAS u32x4*)(d_) = P[0]; *(LAS u32x4*)(d_ + 4096) = P[1]; *(LAS u32x4*)(d_ + 8192) = P[2]; *(LAS u32x4*)(d_ + 12288) = P[3]; } while (0)
#define TDMA4(R, off, dst) do { const unsigned d_ = dk + (dst); dma_nt(R, vk, (off), d_); dma_nt(R, vk, (off) + 4096, d_ + 4096); dma_nt(R, vk, (off) + 8192, d_ + 8192); dma_nt(R, vk, (off) + 12288, d_ + 12288); } while (0)
        int jr = (b + (hl * 5 + 1) / 2) % 10;
        const unsigned plw = lds0 + PL_OFF + 4 * g; unsigned freed = 0; int pub = 0;
#define PUB(cnt) do { if ((int)(cnt) > pub) { pub = (cnt); lds_post(plw, (unsigned)pub); } } while (0)
#define NEED_SLOT(n) do { const int fi_ = ((n) >> 1) - 4; \
            if (fi_ >= 0 && freed <= (unsigned)fi_) { freed = min4(lds_peek4(lds + PC_OFF)); if (freed <= (unsigned)fi_) { \
                VMW(12); PUB((n) - 3); VMW(8); PUB((n) - 2); VMW(4); PUB((n) - 1); VMW(0); PUB(n); \
                AWAITP(freed, PC_OFF, fi_, 2); } } } while (0)
        dma_nt(rK, vk, 0, dk + K0_OFF); dma_nt(rK, vk, 4096, dk + K0_OFF + 4096);
        CHUNK(0); CHUNK(1);
        LBAR();
        int n0 = 20;
        {
            u32x4 k0[4], v0[4], k1[4], v1[4], k2[4], v2[4], k3[4], v3[4];
            if (grpA) {
                PARK4(k0, rK, 8192); VMW(12); PUB(2); PARK4(v0, rV, 0); VMW(8); PUB(4);
                PARK4(k1, rK, 16384 + 8192); PARK4(v1, rV, 16384);
                PARK4(k2, rK, 2 * 16384 + 8192); PARK4(v2, rV, 2 * 16384); PARK4(k3, rK, 3 * 16384 + 8192); PARK4(v3, rV, 3 * 16384);
            } else { VMW(8); PUB(2); }
            for (int j = 2; j < 10; ++j) {
                NEED_SLOT(2 * j);
                XHALF(j & 3); VMW(12); PUB(2 * j - 2);
                WHALF(j & 3); jr = jr == 9 ? 0 : jr + 1; VMW(12); PUB(2 * j - 1);
            }
            if (grpA) {
                VMW(12); PUB(17); VMW(8); PUB(18); VMW(4); PUB(19); VMW(0); PUB(20);
                AWAITP(freed, PC_OFF, 6, 2); UNPARK4(k0, 2 * SLOT); PUB(21); UNPARK4(v0, 2 * SLOT + 16384); PUB(22);
                AWAITP(freed, PC_OFF, 7, 2); UNPARK4(k1, 3 * SLOT); PUB(23); UNPARK4(v1, 3 * SLOT + 16384); PUB(24);
                AWAITP(freed, PC_OFF, 8, 2); UNPARK4(k2, 0); PUB(25); UNPARK4(v2, 16384); PUB(26);
                AWAITP(freed, PC_OFF, 9, 2); UNPARK4(k3, SLOT); PUB(27); UNPARK4(v3, SLOT + 16384); PUB(28);
                n0 = 28;
            }
        }
        u32x4 w0[10], w1[10], w2[10], w3[10]; unsigned vo[5];
#pragma unroll
        for (int pi = 0; pi < 5; ++pi) { const unsigned bi = 4 * pi + (l >> 4), qd = bi / 5, cb = bi - 5 * qd; vo[pi] = (32 * g + 4 * qd + rq) * 1280 + cb * 64 + l3 * 16; }
#define PARKW(W, cc) do { const unsigned so_ = ((hl + (cc)) & 3) * (128 * 1280); _Pragma("unroll") for (int i = 0; i < 10; ++i) W[i] = ld_df(rO, vo[i % 5], so_ + (i / 5) * (16 * 1280)); } while (0)
#define UNPARKW(W, off) do { asm volatile("" : "+v"(W[0]), "+v"(W[1]), "+v"(W[2]), "+v"(W[3]), "+v"(W[4]), "+v"(W[5]), "+v"(W[6]), "+v"(W[7]), "+v"(W[8]), "+v"(W[9])); \
            LAS char* d_ = lds + (off) + g * 10240 + l * 16; _Pragma("unroll") for (int i = 0; i < 10; ++i) *(LAS u32x4*)(d_ + i * 1024) = W[i]; } while (0)
#define KVRANGE(na, nb) for (int n = (na); n < (nb); ++n) { const int u = (n - 20) >> 1, sl = (n >> 1) & 3; \
            if ((n & 1) == 0) { NEED_SLOT(n); TDMA4(rK, u * 16384 + 8192, sl * SLOT); } else TDMA4(rV, u * 16384, sl * SLOT + 16384); \
            VMW(8); PUB(n - 1); }
        KVRANGE(n0, 32); PARKW(w0, 0); KVRANGE(32, 36); PARKW(w1, 1); KVRANGE(36, 40); PARKW(w2, 2); KVRANGE(40, 44); PARKW(w3, 3); KVRANGE(44, 52);
        VMW(4); PUB(51); VMW(0); PUB(52);
        AWAITP(freed, PC_OFF, 23, 1); UNPARKW(w0, 65536); PUB(53); UNPARKW(w1, 106496); PUB(54);
        AWAITP(freed, PC_OFF, 25, 1); UNPARKW(w2, 0); PUB(55);
        if (g == 0) {
            const int p1 = (((b << 2) | (l & 3)) << 3) | xg, p2 = (idx << 3) | (l & 7);
            const unsigned pre1 = __hip_atomic_load((gu32*)(ws + WS_M + p1 * 128), RLX_AGENT), pre2 = __hip_atomic_load((gu32*)(ws + WS_M + p2 * 128), RLX_AGENT);
            const unsigned tok = lds_peek(lds + TOK_OFF);
            AWAITP(freed, PC_OFF, 26, 1);
            handoff(ws, wg, p1, l < 4 && l != hl, hl, l & 3, tok, pre1, lds0 + SY_OFF, 1u);
            UNPARKW(w3, 65536); PUB(56);
            AWAITP(freed, PC_OFF, 27, 1);
            handoff(ws, wg, p2, l < 8 && l != xg, 8 + xg, 8 + (l & 7), tok, pre2, lds0 + SY_OFF, 2u);
            REDUCE(4);
        } else { AWAITP(freed, PC_OFF, 26, 1); UNPARKW(w3, 65536); PUB(56); }
        return;
    }
    LBAR();
    __builtin_amdgcn_s_setprio(3);
    const unsigned pcw = lds0 + PC_OFF + 4 * g; unsigned have = 0;
    if (g == 0) {
        gu32* src = l < 16 ? (gu32*)(ws + WS_R + wg * 128) + l : (gu32*)(ws + WS_M + wg * 128);
        const unsigned v = __hip_atomic_load(src, RLX_AGENT);
        unsigned tok = 1; while (__any(v == tok)) ++tok;
        if (l == 0) __hip_atomic_store((gu32*)(ws + WS_M + wg * 128), tok, RLX_AGENT);
        VMW(0);
        lds_post(lds0 + TOK_OFF, tok);
    }
    bf16x8 qf[8];
    {
        f32x16 qa[4] = {};
        const int row = 32 * g + r;
        const unsigned xb0 = 1024 * (row >> 3) + 64 * (row & 7) + 16 * ((hh) ^ ((row >> 2) & 3));
        const unsigned xb1 = 1024 * (row >> 3) + 64 * (row & 7) + 16 * ((2 + hh) ^ ((row >> 2) & 3));
        const int blk = (l >> 4) & 1, q = (l & 15) >> 2, p = l & 3, pp = ((p & 1) << 1) | (p >> 1);
        const unsigned wb0 = 16384 + 2048 * hh + 64 * q + 16 * ((2 * blk + (pp >> 1)) ^ (2 * hh)) + 8 * (pp & 1);
        const unsigned wb1 = 16384 + 2048 * hh + 1024 + 64 * q + 16 * ((2 * blk + (pp >> 1)) ^ (2 * hh + 1)) + 8 * (pp & 1);
        bf16x8 xA = {}, aA[4] = {}, xB = {}, aB[4] = {}, xC, aC[4], xD, aD[4];
#define WRD(A, ks, db) A[db] = cat(trd(sb + wb0 + 4096 * (ks) + 256 * (db)), trd(sb + wb1 + 4096 * (ks) + 256 * (db)))
#define QSTEP(XO, AO, XN, AN, ks) do { \
            AMM(qa[0], AO[0], XO); XN = *(const LAS bf16x8*)(sb + (((ks) & 1) ? xb1 : xb0) + 512 * ((ks) >> 1)); WRD(AN, ks, 0); SB(); \
            AMM(qa[1], AO[1], XO); WRD(AN, ks, 1); SB(); \
            AMM(qa[2], AO[2], XO); WRD(AN, ks, 2); SB(); \
            AMM(qa[3], AO[3], XO); WRD(AN, ks, 3); SB(); } while (0)
        asm volatile("s_nop 1" : "+v"(qa[0]), "+v"(qa[1]), "+v"(qa[2]), "+v"(qa[3]), "+v"(xA), "+v"(aA[0]), "+v"(aA[1]), "+v"(aA[2]), "+v"(aA[3]),
                     "+v"(xB), "+v"(aB[0]), "+v"(aB[1]), "+v"(aB[2]), "+v"(aB[3]));
        AWAITP(have, PL_OFF, 1, 1); ST(1); STC(43);
        __builtin_amdgcn_s_waitcnt(0xC07F);
        for (int j = 0; j < 10; ++j) {
            if (have <= (unsigned)(2 * j + 1)) AWAITP(have, PL_OFF, 2 * j + 1, 1);
            asm volatile("" ::: "memory");
            const LAS char* sb = lds + (j & 3) * SLOT;
            QSTEP(xA, aA, xC, aC, 0); QSTEP(xB, aB, xD, aD, 1); QSTEP(xC, aC, xA, aA, 2);
            const u32x4 nfv = lds_peek4(lds + PL_OFF); SB();
            QSTEP(xD, aD, xB, aB, 3);
            lds_post(pcw, j + 1);
            have = min4(nfv);
        }
        AMM(qa[0], aA[0], xA); AMM(qa[1], aA[1], xA); AMM(qa[2], aA[2], xA); AMM(qa[3], aA[3], xA);
        AMM(qa[0], aB[0], xB); AMM(qa[1], aB[1], xB); AMM(qa[2], aB[2], xB); AMM(qa[3], aB[3], xB);
        asm volatile("s_nop 15" : "+v"(qa[0]), "+v"(qa[1]), "+v"(qa[2]), "+v"(qa[3]));
        ST(2); STC(44);
#pragma unroll
        for (int db = 0; db < 4; ++db) {
#pragma unroll
            for (int i = 0; i < 16; ++i) qa[db][i] *= QSCALE;
            qf[2 * db] = pack8(qa[db], 0); qf[2 * db + 1] = pack8(qa[db], 8);
        }
    }
    ST(3);
    f32x16 o[4] = {}; f32x16 negm; float m, lsum = 0.f;
    {
        const unsigned kb0 = 1024 * (r >> 2) + 64 * (r & 3) + 16 * ((hh) ^ ((r >> 2) & 3));
        const unsigned kb1 = 1024 * (r >> 2) + 64 * (r & 3) + 16 * ((2 + hh) ^ ((r >> 2) & 3));
        const int blk = (l >> 4) & 1, q = (l & 15) >> 2, p = l & 3;
        const unsigned vb0 = 16384 + 1024 * hh + 64 * q + 16 * ((2 * blk + (p >> 1)) ^ (hh)) + 8 * (p & 1);
        const unsigned vb1 = 16384 + 2048 + 1024 * hh + 64 * q + 16 * ((2 * blk + (p >> 1)) ^ (2 + hh)) + 8 * (p & 1);
        u32x4 pA0, pA1, pB0 = {}, pB1 = {}; bf16x8 kf[8], vf0[4] = {}, vf1[4] = {};
        f32x16 sA = {}, sB;
        {
#pragma unroll
            for (int ks = 0; ks < 8; ++ks) sA = MFMA(*(const LAS bf16x8*)(lds + K0_OFF + ((ks & 1) ? kb1 : kb0) + 256 * (ks >> 1)), qf[ks], sA);
            m = swap32max(max16(sA));
#pragma unroll
            for (int i = 0; i < 16; ++i) { sA[i] -= m; negm[i] = -m; }
        }
#define KRD(ks) kf[ks] = *(const LAS bf16x8*)(sb + (((ks) & 1) ? kb1 : kb0) + 256 * ((ks) >> 1))
#define VRD(V, off) V = cat(trd(sb + vb0 + (off)), trd(sb + vb1 + (off)))
#define SMX(C, P0, P1, i) do { e_[(i) & 3] = EX(C[i]); if ((i) > 0) ps += e_[((i) - 1) & 3]; \
            if (((i) & 1) == 0 && (i) > 0) { const unsigned w_ = cvtpk(e_[((i) - 2) & 3], e_[((i) - 1) & 3]); if ((i) <= 8) { P0[(((i) - 2) >> 1) & 3] = w_; PIN(P0); } else { P1[(((i) - 2) >> 1) & 3] = w_; PIN(P1); } } \
            PIN(e_[(i) & 3]); PIN(ps); } while (0)
#define FIXUP(C, N, P0, P1) do { if (__builtin_expect(__any(!(ps <= PSUM_MAX)), 0)) { \
            asm volatile("s_nop 15" : "+v"(o[0]), "+v"(o[1]), "+v"(o[2]), "+v"(o[3]), "+v"(N)); \
            const float delta_ = fmaxf(swap32max(max16(C)), 0.f), f_ = EX(-delta_); m += delta_; lsum *= f_; ps = 0.f; \
            _Pragma("unroll") for (int i = 0; i < 16; ++i) { C[i] = EX(C[i] - delta_); ps += C[i]; N[i] -= delta_; negm[i] = -m; } \
            P0 = __builtin_bit_cast(u32x4, pack8(C, 0)); P1 = __builtin_bit_cast(u32x4, pack8(C, 8)); \
            _Pragma("unroll") for (int db = 0; db < 4; ++db) _Pragma("unroll") for (int i = 0; i < 16; ++i) o[db][i] *= f_; \
            asm volatile("s_nop 1" : "+v"(o[0]), "+v"(o[1]), "+v"(o[2]), "+v"(o[3]), "+v"(negm), "+v"(P0), "+v"(P1)); } } while (0)
#define STEP(C, N, P0, P1, Q0, Q1, MID, LATE) do { float ps = 0.f, e_[4]; \
            AMM(o[0], vf0[0], Q0); SB(); KRD(0); KRD(1); SB(); \
            AMM(o[1], vf0[1], Q0); SB(); KRD(2); KRD(3); SB(); \
            AMM(o[2], vf0[2], Q0); SB(); KRD(4); KRD(5); asm volatile("" : "+v"(C)); SMX(C, P0, P1, 0); SB(); \
            AMM(o[3], vf0[3], Q0); SB(); KRD(6); KRD(7); SMX(C, P0, P1, 1); SB(); \
            MID; \
            AMM(o[0], vf1[0], Q1); SB(); VRD(vf0[0], 0); SMX(C, P0, P1, 2); SB(); \
            AMM(o[1], vf1[1], Q1); SB(); VRD(vf0[1], 256); SMX(C, P0, P1, 3); SB(); \
            AMM(o[2], vf1[2], Q1); SB(); VRD(vf0[2], 512); SMX(C, P0, P1, 4); SB(); \
            AMM(o[3], vf1[3], Q1); SB(); VRD(vf0[3], 768); SMX(C, P0, P1, 5); SB(); \
            AMM0(N, kf[0], qf[0], negm); SB(); VRD(vf1[0], 4096); SMX(C, P0, P1, 6); SB(); \
            AMM(N, kf[1], qf[1]); SB(); VRD(vf1[1], 4096 + 256); SMX(C, P0, P1, 7); SB(); \
            AMM(N, kf[2], qf[2]); SB(); VRD(vf1[2], 4096 + 512); SMX(C, P0, P1, 8); SB(); \
            AMM(N, kf[3], qf[3]); SB(); VRD(vf1[3], 4096 + 768); SMX(C, P0, P1, 9); SB(); \
            AMM(N, kf[4], qf[4]); SB(); LATE; SMX(C, P0, P1, 10); SMX(C, P0, P1, 11); SB(); \
            AMM(N, kf[5], qf[5]); SB(); SMX(C, P0, P1, 12); SMX(C, P0, P1, 13); SB(); \
            AMM(N, kf[6], qf[6]); SB(); SMX(C, P0, P1, 14); SMX(C, P0, P1, 15); SB(); \
            AMM(N, kf[7], qf[7]); SB(); ps += e_[3]; P1[3] = cvtpk(e_[2], e_[3]); PIN(P1); PIN(ps); SB(); \
            FIXUP(C, N, P0, P1); \
            lsum += ps; } while (0)
        asm volatile("s_nop 1" : "+v"(o[0]), "+v"(o[1]), "+v"(o[2]), "+v"(o[3]), "+v"(negm), "+v"(pB0), "+v"(pB1), "+v"(vf0[0]), "+v"(vf0[1]), "+v"(vf0[2]), "+v"(vf0[3]),
                     "+v"(vf1[0]), "+v"(vf1[1]), "+v"(vf1[2]), "+v"(vf1[3]));
        ST(4); STC(41);
        __builtin_amdgcn_s_waitcnt(0xC07F);
        for (int u = 0; u < 15; ++u) {
            if (have <= (unsigned)(20 + 2 * u)) AWAITP(have, PL_OFF, 20 + 2 * u, 1);
            asm volatile("" ::: "memory");
            u32x4 nfv;
            { const LAS char* sb = lds + ((u + 2) & 3) * SLOT;        STEP(sA, sB, pA0, pA1, pB0, pB1, if (have <= (unsigned)(21 + 2 * u)) AWAITP(have, PL_OFF, 21 + 2 * u, 1), (void)0); }
            { const LAS char* sb = lds + ((u + 2) & 3) * SLOT + 8192; STEP(sB, sA, pB0, pB1, pA0, pA1, (void)0,
                  { lds_post(pcw, 11 + u); nfv = lds_peek4(lds + PL_OFF); }); }
            have = min4(nfv);
        }
        ST(5); STC(42);
        {
            const LAS char* sb = lds + 1 * SLOT;
            if (have <= 50u) AWAITP(have, PL_OFF, 50, 1);
            { float ps = 0.f, e_[4];
              AMM(o[0], vf0[0], pB0); SB(); KRD(0); KRD(1); SB();
              AMM(o[1], vf0[1], pB0); SB(); KRD(2); KRD(3); SB();
              AMM(o[2], vf0[2], pB0); SB(); KRD(4); KRD(5); asm volatile("" : "+v"(sA)); SMX(sA, pA0, pA1, 0); SB();
              AMM(o[3], vf0[3], pB0); SB(); KRD(6); KRD(7); SMX(sA, pA0, pA1, 1); SB();
              AMM(o[0], vf1[0], pB1); SB(); SMX(sA, pA0, pA1, 2); SB();
              AMM(o[1], vf1[1], pB1); SB(); SMX(sA, pA0, pA1, 3); SB();
              AMM(o[2], vf1[2], pB1); SB(); SMX(sA, pA0, pA1, 4); SB();
              AMM(o[3], vf1[3], pB1); SB(); SMX(sA, pA0, pA1, 5); SB();
              AMM0(sB, kf[0], qf[0], negm); SB(); SMX(sA, pA0, pA1, 6); SB();
              AMM(sB, kf[1], qf[1]); SB(); SMX(sA, pA0, pA1, 7); SB();
              AMM(sB, kf[2], qf[2]); SB(); SMX(sA, pA0, pA1, 8); SB();
              AMM(sB, kf[3], qf[3]); SB(); SMX(sA, pA0, pA1, 9); SB();
              AMM(sB, kf[4], qf[4]); SB(); SMX(sA, pA0, pA1, 10); SMX(sA, pA0, pA1, 11); SB();
              AMM(sB, kf[5], qf[5]); SB(); SMX(sA, pA0, pA1, 12); SMX(sA, pA0, pA1, 13); SB();
              AMM(sB, kf[6], qf[6]); SB(); SMX(sA, pA0, pA1, 14); SMX(sA, pA0, pA1, 15); SB();
              AMM(sB, kf[7], qf[7]); SB(); ps += e_[3]; pA1[3] = cvtpk(e_[2], e_[3]); PIN(pA1); PIN(ps); SB();
              FIXUP(sA, sB, pA0, pA1);
              lsum += ps; }
            asm volatile("s_nop 15" : "+v"(sB));
            { const float delta_ = ceilf(fmaxf(swap32max(max16(sB)) - 8.f, 0.f));
              if (__builtin_expect(__any(delta_ > 0.f), 0)) {
                  const float f_ = EX(-delta_); m += delta_; lsum *= f_;
#pragma unroll
                  for (int i = 0; i < 4; ++i) { pA0[i] = cvtpk(__uint_as_float(pA0[i] << 16) * f_, __uint_as_float(pA0[i] & 0xffff0000u) * f_);
                                                pA1[i] = cvtpk(__uint_as_float(pA1[i] << 16) * f_, __uint_as_float(pA1[i] & 0xffff0000u) * f_); }
#pragma unroll
                  for (int i = 0; i < 16; ++i) sB[i] -= delta_;
#pragma unroll
                  for (int db = 0; db < 4; ++db)
#pragma unroll
                      for (int i = 0; i < 16; ++i) o[db][i] *= f_;
              }
              float ps = 0.f;
#pragma unroll
              for (int i = 0; i < 16; ++i) { sB[i] = EX(sB[i]); ps += sB[i]; }
              lsum += ps;
              pB0 = __builtin_bit_cast(u32x4, pack8(sB, 0)); pB1 = __builtin_bit_cast(u32x4, pack8(sB, 8));
              asm volatile("s_nop 1" : "+v"(o[0]), "+v"(o[1]), "+v"(o[2]), "+v"(o[3]), "+v"(pA0), "+v"(pA1), "+v"(pB0), "+v"(pB1), "+v"(lsum)); }
            if (have <= 51u) AWAITP(have, PL_OFF, 51, 1);
            ST(6);
            bf16x8 vg0[4], vg1[4];
            VRD(vf0[0], 0); VRD(vf0[1], 256); VRD(vf0[2], 512); VRD(vf0[3], 768);
            VRD(vf1[0], 4096); VRD(vf1[1], 4096 + 256); VRD(vf1[2], 4096 + 512); VRD(vf1[3], 4096 + 768); SB();
            AMM(o[0], vf0[0], pA0); VRD(vg0[0], 8192); SB(); AMM(o[1], vf0[1], pA0); VRD(vg0[1], 8192 + 256); SB();
            AMM(o[2], vf0[2], pA0); VRD(vg0[2], 8192 + 512); SB(); AMM(o[3], vf0[3], pA0); VRD(vg0[3], 8192 + 768); SB();
            AMM(o[0], vf1[0], pA1); VRD(vg1[0], 12288); SB(); AMM(o[1], vf1[1], pA1); VRD(vg1[1], 12288 + 256); SB();
            AMM(o[2], vf1[2], pA1); VRD(vg1[2], 12288 + 512); SB(); AMM(o[3], vf1[3], pA1); VRD(vg1[3], 12288 + 768); SB();
            AMM(o[0], vg0[0], pB0); AMM(o[1], vg0[1], pB0); AMM(o[2], vg0[2], pB0); AMM(o[3], vg0[3], pB0);
            AMM(o[0], vg1[0], pB1); AMM(o[1], vg1[1], pB1); AMM(o[2], vg1[2], pB1); AMM(o[3], vg1[3], pB1);
            asm volatile("s_nop 15" : "+v"(o[0]), "+v"(o[1]), "+v"(o[2]), "+v"(o[3]));
        }
    }
    {
        ST(7);
        lds_post(pcw, 26);
        const float inv = 1.0f / swap32sum(lsum);
        bf16x8 of[8];
#pragma unroll
        for (int db = 0; db < 4; ++db) {
#pragma unroll
            for (int i = 0; i < 16; ++i) o[db][i] *= inv;
            of[2 * db] = pack8(o[db], 0); of[2 * db + 1] = pack8(o[db], 8);
        }
#pragma unroll
        for (int s = 0; s < 8; ++s) __builtin_amdgcn_raw_buffer_store_b128(__builtin_bit_cast(u32x4, of[s]), rWS, l * 16, WS_O + ((wg * 4 + g) * 8 + s) * 1024, 16);
        const int blk = (l >> 4) & 1, q = (l & 15) >> 2, p = l & 3;
        const unsigned ob = 1280 * hh + 64 * q + 16 * (2 * blk + (p >> 1)) + 8 * (p & 1);
        f32x16 acc[5] = {};
        bf16x8 aA[5], aB[5];
        asm volatile("s_nop 1" : "+v"(acc[0]), "+v"(acc[1]), "+v"(acc[2]), "+v"(acc[3]), "+v"(acc[4]), "+v"(of[0]), "+v"(of[1]), "+v"(of[2]), "+v"(of[3]), "+v"(of[4]), "+v"(of[5]), "+v"(of[6]), "+v"(of[7]));
#define ORD(A, s, nb) A[nb] = cat(trd(wb + 5120 * (s) + 256 * (nb)), trd(wb + 5120 * (s) + 2560 + 256 * (nb)))
#define OSTEP(AO, AN, F, sn) do { \
            AMM(acc[0], AO[0], F); if ((sn) < 8) ORD(AN, (sn) & 7, 0); SB(); AMM(acc[1], AO[1], F); if ((sn) < 8) ORD(AN, (sn) & 7, 1); SB(); AMM(acc[2], AO[2], F); if ((sn) < 8) ORD(AN, (sn) & 7, 2); SB(); \
            AMM(acc[3], AO[3], F); if ((sn) < 8) ORD(AN, (sn) & 7, 3); SB(); AMM(acc[4], AO[4], F); if ((sn) < 8) ORD(AN, (sn) & 7, 4); SB(); } while (0)
#define OHEAD(F, off) do { const LAS char* wb = lds + (off) + ob; ORD(aA, 0, 0); ORD(aA, 0, 1); ORD(aA, 0, 2); ORD(aA, 0, 3); ORD(aA, 0, 4); SB(); \
            OSTEP(aA, aB, F[0], 1); OSTEP(aB, aA, F[1], 2); OSTEP(aA, aB, F[2], 3); OSTEP(aB, aA, F[3], 4); OSTEP(aA, aB, F[4], 5); OSTEP(aB, aA, F[5], 6); OSTEP(aA, aB, F[6], 7); OSTEP(aB, aA, F[7], 8); } while (0)
        AWAITP(have, PL_OFF, 52, 1);
        OHEAD(of, 65536);
        VMW(0);
        lds_post(pcw, 27);
        ST(9);
        AWAIT1(SY_OFF, 1);
        ST(10);
        bf16x8 f1[8], f2[8], f3[8];
#define OLOAD(F, cc) do { const unsigned pw_ = (((b << 2) | ((hl + (cc)) & 3)) << 3) | xg; _Pragma("unroll") for (int s = 0; s < 8; ++s) \
            F[s] = __builtin_bit_cast(bf16x8, __builtin_amdgcn_raw_buffer_load_b128(rWS, l * 16, WS_O + ((pw_ * 4 + g) * 8 + s) * 1024, 16)); } while (0)
        OLOAD(f1, 1); OLOAD(f2, 2); OLOAD(f3, 3);
        AWAITP(have, PL_OFF, 54, 1);
        OHEAD(f1, 106496); OHEAD(f2, 0);
        AWAITP(have, PL_OFF, 55, 1);
        OHEAD(f3, 65536);
        asm volatile("s_nop 15" : "+v"(acc[0]), "+v"(acc[1]), "+v"(acc[2]), "+v"(acc[3]), "+v"(acc[4]));
        ST(11);
#pragma unroll
        for (int u = 0; u < 10; ++u) __builtin_amdgcn_raw_buffer_store_b128(__builtin_bit_cast(u32x4, pack8(acc[u >> 1], 8 * (u & 1))), rWS, l * 16, WS_P + (wg * 40 + g * 10 + u) * 1024, 16);
        VMW(0);
        lds_post(pcw, 28);
        ST(12);
        AWAIT1(SY_OFF, 2);
        ST(13);
        REDUCE(g);
        ST(8);
    }
}

extern "C" void kernel_launch(void* const* d_in, const int* in_sizes, int n_in, void* d_out, int out_size, void* d_ws, size_t ws_size, hipStream_t stream) {
    (void)hipFuncSetAttribute((const void*)k_fused, hipFuncAttributeMaxDynamicSharedMemorySize, LDS_BYTES);
    k_fused<<<256, 512, LDS_BYTES, stream>>>((const char*)d_in[0], (const char*)d_in[1], (const char*)d_in[2], (const char*)d_in[3], (const char*)d_in[4], (char*)d_out, (char*)d_ws);
}
```

```cpp
#include <hip/hip_runtime.h>
#include <hip/hip_bf16.h>

typedef short bf16x8 __attribute__((ext_vector_type(8)));
typedef short s16x4 __attribute__((ext_vector_type(4)));
typedef float f32x16 __attribute__((ext_vector_type(16)));
typedef float f32x4 __attribute__((ext_vector_type(4)));
typedef unsigned u32x4 __attribute__((ext_vector_type(4)));
typedef unsigned u32x2 __attribute__((ext_vector_type(2)));
typedef __bf16 bf16x2_t __attribute__((ext_vector_type(2)));
typedef float f32x2 __attribute__((ext_vector_type(2)));
#define LAS __attribute__((address_space(3)))
#define DI __device__ __forceinline__
#define MFMA(a, b, c) __builtin_amdgcn_mfma_f32_32x32x16_bf16((a), (b), (c), 0, 0, 0)

constexpr int SLOT = 32768;
constexpr int LDS_BYTES = 163840;
constexpr float QSCALE = 0.08838834764831845f * 1.4426950408889634f;
constexpr float PSUM_MAX = 4096.0f;
constexpr unsigned WS_O = 0;
constexpr unsigned WS_P = 8u << 20;
constexpr unsigned WS_M = 20u << 20;
constexpr unsigned WS_R = WS_M + 32768;

DI unsigned cvtpk(float lo, float hi) { f32x2 v = {lo, hi}; bf16x2_t b = __builtin_convertvector(v, bf16x2_t); return __builtin_bit_cast(unsigned, b); }
DI bf16x8 pack8(const f32x16& x, int base) {
    u32x4 w = {cvtpk(x[base + 0], x[base + 1]), cvtpk(x[base + 2], x[base + 3]), cvtpk(x[base + 4], x[base + 5]), cvtpk(x[base + 6], x[base + 7])};
    return __builtin_bit_cast(bf16x8, w);
}
DI s16x4 trd(const LAS char* p) { return __builtin_bit_cast(s16x4, __builtin_amdgcn_ds_read_tr16_b64_v4i16((LAS s16x4*)p)); }
DI bf16x8 cat(s16x4 a, s16x4 b) { return __builtin_shufflevector(a, b, 0, 1, 2, 3, 4, 5, 6, 7); }
DI float swap32max(float v) { auto r = __builtin_amdgcn_permlane32_swap(__float_as_uint(v), __float_as_uint(v), false, false); return fmaxf(__uint_as_float(r[0]), __uint_as_float(r[1])); }
DI float swap32sum(float v) { auto r = __builtin_amdgcn_permlane32_swap(__float_as_uint(v), __float_as_uint(v), false, false); return __uint_as_float(r[0]) + __uint_as_float(r[1]); }
DI float max16(const f32x16& s) {
    float a = fmaxf(fmaxf(s[0], s[1]), s[2]), b = fmaxf(fmaxf(s[3], s[4]), s[5]);
    a = fmaxf(fmaxf(a, s[6]), s[7]); b = fmaxf(fmaxf(b, s[8]), s[9]); a = fmaxf(fmaxf(a, s[10]), s[11]); b = fmaxf(fmaxf(b, s[12]), s[13]);
    return fmaxf(fmaxf(fmaxf(a, s[14]), s[15]), b);
}
DI void dma_nt(__amdgpu_buffer_rsrc_t r, unsigned voff, unsigned soff, unsigned ldsaddr) {
    asm volatile("s_mov_b32 m0, %3\n\ts_nop 0\n\tbuffer_load_dwordx4 %0, %1, %2 offen nt lds" :: "v"(voff), "s"(r), "s"(soff), "s"(ldsaddr) : "memory");
}
DI void dma_df(__amdgpu_buffer_rsrc_t r, unsigned voff, unsigned soff, unsigned ldsaddr) {
    asm volatile("s_mov_b32 m0, %3\n\ts_nop 0\n\tbuffer_load_dwordx4 %0, %1, %2 offen lds" :: "v"(voff), "s"(r), "s"(soff), "s"(ldsaddr) : "memory");
}
DI u32x4 ld_df(__amdgpu_buffer_rsrc_t r, unsigned voff, unsigned soff) {
    u32x4 v; asm volatile("buffer_load_dwordx4 %0, %1, %2, %3 offen" : "=v"(v) : "v"(voff), "s"(r), "s"(soff) : "memory"); return v;
}
DI u32x4 ld_nt(__amdgpu_buffer_rsrc_t r, unsigned voff, unsigned soff) {
    u32x4 v; asm volatile("buffer_load_dwordx4 %0, %1, %2, %3 offen nt" : "=v"(v) : "v"(voff), "s"(r), "s"(soff) : "memory"); return v;
}
#define ST(k)
#define STC(k)
#define WAIT_BAR(N) asm volatile("s_waitcnt vmcnt(" #N ") lgkmcnt(0)\n\ts_barrier" ::: "memory")
#define LBAR() asm volatile("s_waitcnt lgkmcnt(0)\n\ts_barrier" ::: "memory")
#define PIN4(a) asm volatile("" : "+v"(a[0]), "+v"(a[1]), "+v"(a[2]), "+v"(a[3]))
#define SB() __builtin_amdgcn_sched_barrier(0)
#define PIN(x) asm volatile("" : "+v"(x))
#define EX(x) __builtin_amdgcn_exp2f(x)
#define AMM(D, A, B) asm volatile("v_mfma_f32_32x32x16_bf16 %0, %1, %2, %0" : "+v"(D) : "v"(A), "v"(B))
#define AMM0(D, A, B, C0) asm volatile("v_mfma_f32_32x32x16_bf16 %0, %1, %2, %3" : "=&v"(D) : "v"(A), "v"(B), "v"(C0))
DI __amdgpu_buffer_rsrc_t mkrsrc(const void* p, unsigned bytes) { return __builtin_amdgcn_make_buffer_rsrc((void*)p, 0, bytes, 0x00020000); }

constexpr int K0_OFF = 131072, CTL_OFF = 147456;
constexpr int PL_OFF = CTL_OFF, PC_OFF = CTL_OFF + 64, SY_OFF = CTL_OFF + 128, TOK_OFF = CTL_OFF + 132;
DI void lds_post(unsigned addr, unsigned v) { asm volatile("s_mov_b64 exec, 1\n\tds_write_b32 %0, %1\n\ts_mov_b64 exec, -1" :: "v"(addr), "v"(v) : "memory"); }
DI u32x4 lds_peek4(const LAS char* p) { return *(const volatile LAS u32x4*)p; }
DI unsigned lds_peek(const LAS char* p) { return __builtin_amdgcn_readfirstlane(*(const volatile LAS unsigned*)p); }
DI unsigned min4(u32x4 v) { return __builtin_amdgcn_readfirstlane(min(min(v[0], v[1]), min(v[2], v[3]))); }
typedef __attribute__((address_space(1))) unsigned gu32;
#define RLX_AGENT __ATOMIC_RELAXED, __HIP_MEMORY_SCOPE_AGENT
constexpr int SPIN_CAP = 1 << 18;

DI void handoff(char* ws, int me, int pw, bool act, int myslot, int pslot, unsigned tok, unsigned pre, unsigned syaddr, unsigned syval) {
    gu32* pm = (gu32*)(ws + WS_M + pw * 128); gu32* pr = (gu32*)(ws + WS_R + pw * 128) + myslot; gu32* mr = (gu32*)(ws + WS_R + me * 128) + pslot;
    unsigned last = pre;
    if (act) __hip_atomic_store(pr, pre, RLX_AGENT);
    for (int sp = 0; sp < SPIN_CAP; ++sp) {
        unsigned rj = tok;
        if (act) rj = __hip_atomic_load(mr, RLX_AGENT);
        if (__all(rj == tok)) break;
        if ((sp & 3) == 3 && act) { const unsigned mj = __hip_atomic_load(pm, RLX_AGENT); if (mj != last) { __hip_atomic_store(pr, mj, RLX_AGENT); last = mj; } }
        __builtin_amdgcn_s_sleep(1);
    }
    asm volatile("" ::: "memory");
    lds_post(syaddr, syval);
    if (act) { const unsigned mj = __hip_atomic_load(pm, RLX_AGENT); if (mj != last) __hip_atomic_store(pr, mj, RLX_AGENT); }
}

__global__ __launch_bounds__(512, 2) void k_fused(const char* __restrict__ x, const char* __restrict__ K, const char* __restrict__ V,
                                                  const char* __restrict__ wq, const char* __restrict__ wo, char* __restrict__ out, char* __restrict__ ws) {
    extern __shared__ __attribute__((aligned(16))) char lds_[];
    LAS char* lds = (LAS char*)lds_;
    const unsigned lds0 = (unsigned)(size_t)lds;
    const int wg = blockIdx.x, xg = wg & 7, idx = wg >> 3, b = idx >> 2, hl = idx & 3, h = xg * 4 + hl;
    const int tid = threadIdx.x, l = tid & 63, w = __builtin_amdgcn_readfirstlane(tid >> 6), g = w & 3, c = w >> 2;
    const int r = l & 31, hh = l >> 5;
    const size_t bh = (size_t)(b * 32 + h);
    const __amdgpu_buffer_rsrc_t rWS = mkrsrc(ws, 20u << 20);
    ST(0);
    if (tid < 40) *(LAS unsigned*)(lds + CTL_OFF + tid * 4) = 0u;
#define AWAITP(have, off, n, SL) do { int sp_ = 0; while ((have = min4(lds_peek4(lds + (off)))) <= (unsigned)(n) && ++sp_ < SPIN_CAP) __builtin_amdgcn_s_sleep(SL); asm volatile("" ::: "memory"); } while (0)
#define AWAIT1(off, n) do { int sp_ = 0; while (lds_peek(lds + (off)) < (unsigned)(n) && ++sp_ < SPIN_CAP) __builtin_amdgcn_s_sleep(1); asm volatile("" ::: "memory"); } while (0)
#define VMW(N) asm volatile("s_waitcnt vmcnt(" #N ")" ::: "memory")
#define REDUCE(un) do { const int ui_ = 5 * xg + (un), ug_ = ui_ / 10, uu_ = ui_ % 10; u32x4 pv_[8]; \
        _Pragma("unroll") for (int j = 0; j < 8; ++j) pv_[j] = __builtin_amdgcn_raw_buffer_load_b128(rWS, l * 16, WS_P + ((idx * 8 + j) * 40 + ui_) * 1024, 16); \
        float a_[8] = {}; \
        _Pragma("unroll") for (int j = 0; j < 8; ++j) _Pragma("unroll") for (int e = 0; e < 4; ++e) { a_[2 * e] += __uint_as_float(pv_[j][e] << 16); a_[2 * e + 1] += __uint_as_float(pv_[j][e] & 0xffff0000u); } \
        char* o_ = out + ((size_t)(b * 128 + 32 * ug_ + r) * 640 + 160 * hl + 16 * uu_ + 4 * hh) * 2; \
        *(u32x2*)o_ = (u32x2){cvtpk(a_[0], a_[1]), cvtpk(a_[2], a_[3])}; *(u32x2*)(o_ + 16) = (u32x2){cvtpk(a_[4], a_[5]), cvtpk(a_[6], a_[7])}; } while (0)
    if (c == 1) {
        const __amdgpu_buffer_rsrc_t rK = mkrsrc(K + bh * 262144, 262144), rV = mkrsrc(V + bh * 262144, 262144);
        const __amdgpu_buffer_rsrc_t rX = mkrsrc(x + (size_t)b * 163840, 163840), rW = mkrsrc(wq + h * 256, 640 * 8192 - h * 256);
        const __amdgpu_buffer_rsrc_t rO = mkrsrc(wo + (size_t)xg * 512 * 1280 + hl * 320, 512 * 1280 - hl * 320);
#define GRPM 0
        const bool grpA = GRPM == 0 ? (((b ^ h) & 1) != 0) : (GRPM == 1);
        const unsigned lrow = (l >> 2) & 7, lsub = (l >> 5) * 64, l4 = (l >> 4) & 1, l3 = l & 3, rq = (l >> 2) & 3, csub = (l >> 4) * 64;
        const unsigned vk = (4 * g + rq) * 256 + csub + ((l3 ^ g) * 16), vw = (4 * g + rq) * 8192 + csub + ((l3 ^ g) * 16);
        const unsigned vx0 = (32 * g + lrow) * 1280 + lsub + ((l3 ^ l4) * 16), vx1 = (32 * g + 8 + lrow) * 1280 + lsub + ((l3 ^ (2 + l4)) * 16);
        const unsigned dk = lds0 + g * 1024, dx = lds0 + 4 * g * 1024;
#define XHALF(sl) do { const unsigned d_ = dx + (sl) * SLOT, sx_ = jr * 128; \
        dma_df(rX, vx0, sx_, d_); dma_df(rX, vx1, sx_, d_ + 1024); dma_df(rX, vx0, sx_ + 16 * 1280, d_ + 2048); dma_df(rX, vx1, sx_ + 16 * 1280, d_ + 3072); } while (0)
#define WHALF(sl) do { const unsigned e_ = dk + (sl) * SLOT + 16384, sq_ = jr * (64 * 8192); \
        dma_nt(rW, vw, sq_, e_); dma_nt(rW, vw, sq_ + 16 * 8192, e_ + 4096); dma_nt(rW, vw, sq_ + 32 * 8192, e_ + 8192); dma_nt(rW, vw, sq_ + 48 * 8192, e_ + 12288); } while (0)
#define CHUNK(sl) do { XHALF(sl); WHALF(sl); jr = jr == 9 ? 0 : jr + 1; } while (0)
#define PARK4(P, R, off) do { P[0] = ld_nt(R, vk, (off)); P[1] = ld_nt(R, vk, (off) + 4096); P[2] = ld_nt(R, vk, (off) + 8192); P[3] = ld_nt(R, vk, (off) + 12288); } while (0)
#define UNPARK4(P, off) do { PIN4(P); LAS char* d_ = lds + (off) + g * 1024 + l * 16; *(LAS u32x4*)(d_) = P[0]; *(LAS u32x4*)(d_ + 4096) = P[1]; *(LAS u32x4*)(d_ + 8192) = P[2]; *(LAS u32x4*)(d_ + 12288) = P[3]; } while (0)
#define TDMA4(R, off, dst) do { const unsigned d_ = dk + (dst); dma_nt(R, vk, (off), d_); dma_nt(R, vk, (off) + 4096, d_ + 4096); dma_nt(R, vk, (off) + 8192, d_ + 8192); dma_nt(R, vk, (off) + 12288, d_ + 12288); } while (0)
        int jr = (b + (hl * 5 + 1) / 2) % 10;
        const unsigned plw = lds0 + PL_OFF + 4 * g; unsigned freed = 0; int pub = 0;
#define PUB(cnt) do { if ((int)(cnt) > pub) { pub = (cnt); lds_post(plw, (unsigned)pub); } } while (0)
#define NEED_SLOT(n) do { const int fi_ = ((n) >> 1) - 4; \
            if (fi_ >= 0 && freed <= (unsigned)fi_) { freed = min4(lds_peek4(lds + PC_OFF)); if (freed <= (unsigned)fi_) { \
                VMW(12); PUB((n) - 3); VMW(8); PUB((n) - 2); VMW(4); PUB((n) - 1); VMW(0); PUB(n); \
                AWAITP(freed, PC_OFF, fi_, 2); } } } while (0)
        dma_nt(rK, vk, 0, dk + K0_OFF); dma_nt(rK, vk, 4096, dk + K0_OFF + 4096);
        CHUNK(0); CHUNK(1);
        LBAR();
        int n0 = 20;
        {
            u32x4 k0[4], v0[4], k1[4], v1[4], k2[4], v2[4], k3[4], v3[4];
            if (grpA) {
                PARK4(k0, rK, 8192); VMW(12); PUB(2); PARK4(v0, rV, 0); VMW(8); PUB(4);
                PARK4(k1, rK, 16384 + 8192); PARK4(v1, rV, 16384);
                PARK4(k2, rK, 2 * 16384 + 8192); PARK4(v2, rV, 2 * 16384); PARK4(k3, rK, 3 * 16384 + 8192); PARK4(v3, rV, 3 * 16384);
            } else { VMW(8); PUB(2); }
            for (int j = 2; j < 10; ++j) {
                NEED_SLOT(2 * j);
                XHALF(j & 3); VMW(12); PUB(2 * j - 2);
                WHALF(j & 3); jr = jr == 9 ? 0 : jr + 1; VMW(12); PUB(2 * j - 1);
            }
            if (grpA) {
                VMW(12); PUB(17); VMW(8); PUB(18); VMW(4); PUB(19); VMW(0); PUB(20);
                AWAITP(freed, PC_OFF, 6, 2); UNPARK4(k0, 2 * SLOT); PUB(21); UNPARK4(v0, 2 * SLOT + 16384); PUB(22);
                AWAITP(freed, PC_OFF, 7, 2); UNPARK4(k1, 3 * SLOT); PUB(23); UNPARK4(v1, 3 * SLOT + 16384); PUB(24);
                AWAITP(freed, PC_OFF, 8, 2); UNPARK4(k2, 0); PUB(25); UNPARK4(v2, 16384); PUB(26);
                AWAITP(freed, PC_OFF, 9, 2); UNPARK4(k3, SLOT); PUB(27); UNPARK4(v3, SLOT + 16384); PUB(28);
                n0 = 28;
            }
        }
        u32x4 w0[10], w1[10], w2[10], w3[10]; unsigned vo[5];
#pragma unroll
        for (int pi = 0; pi < 5; ++pi) { const unsigned bi = 4 * pi + (l >> 4), qd = bi / 5, cb = bi - 5 * qd; vo[pi] = (32 * g + 4 * qd + rq) * 1280 + cb * 64 + l3 * 16; }
#define LDWO ld_nt
#define PARKW(W, cc) do { const unsigned so_ = ((hl + (cc)) & 3) * (128 * 1280); _Pragma("unroll") for (int i = 0; i < 10; ++i) W[i] = LDWO(rO, vo[i % 5], so_ + (i / 5) * (16 * 1280)); } while (0)
#define UNPARKW(W, off) do { asm volatile("" : "+v"(W[0]), "+v"(W[1]), "+v"(W[2]), "+v"(W[3]), "+v"(W[4]), "+v"(W[5]), "+v"(W[6]), "+v"(W[7]), "+v"(W[8]), "+v"(W[9])); \
            LAS char* d_ = lds + (off) + g * 10240 + l * 16; _Pragma("unroll") for (int i = 0; i < 10; ++i) *(LAS u32x4*)(d_ + i * 1024) = W[i]; } while (0)
#define KVW VMW(8); PUB(n - 1)
#define KVRANGE(na, nb) for (int n = (na); n < (nb); ++n) { const int u = (n - 20) >> 1, sl = (n >> 1) & 3; \
            if ((n & 1) == 0) { NEED_SLOT(n); TDMA4(rK, u * 16384 + 8192, sl * SLOT); } else TDMA4(rV, u * 16384, sl * SLOT + 16384); \
            KVW; }
        KVRANGE(n0, 32); PARKW(w0, 0); KVRANGE(32, 36); PARKW(w1, 1); KVRANGE(36, 40); PARKW(w2, 2); KVRANGE(40, 44); PARKW(w3, 3); KVRANGE(44, 52);
        VMW(12); PUB(49); VMW(8); PUB(50); VMW(4); PUB(51); VMW(0); PUB(52);
        AWAITP(freed, PC_OFF, 23, 1); UNPARKW(w0, 65536); PUB(53); UNPARKW(w1, 106496); PUB(54);
        AWAITP(freed, PC_OFF, 25, 1); UNPARKW(w2, 0); PUB(55);
        if (g == 0) {
            const int p1 = (((b << 2) | (l & 3)) << 3) | xg, p2 = (idx << 3) | (l & 7);
            const unsigned pre1 = __hip_atomic_load((gu32*)(ws + WS_M + p1 * 128), RLX_AGENT), pre2 = __hip_atomic_load((gu32*)(ws + WS_M + p2 * 128), RLX_AGENT);
            AWAITP(freed, PC_OFF, 26, 1);
            const unsigned tok = lds_peek(lds + TOK_OFF);
            handoff(ws, wg, p1, l < 4 && l != hl, hl, l & 3, tok, pre1, lds0 + SY_OFF, 1u);
            UNPARKW(w3, 65536); PUB(56);
            AWAITP(freed, PC_OFF, 27, 1);
            handoff(ws, wg, p2, l < 8 && l != xg, 8 + xg, 8 + (l & 7), tok, pre2, lds0 + SY_OFF, 2u);
            REDUCE(4);
        } else { AWAITP(freed, PC_OFF, 26, 1); UNPARKW(w3, 65536); PUB(56); }
        return;
    }
    LBAR();
    __builtin_amdgcn_s_setprio(3);
    const unsigned pcw = lds0 + PC_OFF + 4 * g; unsigned have = 0;
    unsigned tokv = 0;
    if (g == 0) tokv = __hip_atomic_load(l < 16 ? (gu32*)(ws + WS_R + wg * 128) + l : (gu32*)(ws + WS_M + wg * 128), RLX_AGENT);
    bf16x8 qf[8];
    {
        f32x16 qa[4] = {};
        const int row = 32 * g + r;
        const unsigned xb0 = 1024 * (row >> 3) + 64 * (row & 7) + 16 * ((hh) ^ ((row >> 2) & 3));
        const unsigned xb1 = 1024 * (row >> 3) + 64 * (row & 7) + 16 * ((2 + hh) ^ ((row >> 2) & 3));
        const int blk = (l >> 4) & 1, q = (l & 15) >> 2, p = l & 3, pp = ((p & 1) << 1) | (p >> 1);
        const unsigned wb0 = 16384 + 2048 * hh + 64 * q + 16 * ((2 * blk + (pp >> 1)) ^ (2 * hh)) + 8 * (pp & 1);
        const unsigned wb1 = 16384 + 2048 * hh + 1024 + 64 * q + 16 * ((2 * blk + (pp >> 1)) ^ (2 * hh + 1)) + 8 * (pp & 1);
        bf16x8 xA = {}, aA[4] = {}, xB = {}, aB[4] = {}, xC, aC[4], xD, aD[4];
#define WRD(A, ks, db) A[db] = cat(trd(sb + wb0 + 4096 * (ks) + 256 * (db)), trd(sb + wb1 + 4096 * (ks) + 256 * (db)))
#define QSTEP(XO, AO, XN, AN, ks) do { \
            AMM(qa[0], AO[0], XO); XN = *(const LAS bf16x8*)(sb + (((ks) & 1) ? xb1 : xb0) + 512 * ((ks) >> 1)); WRD(AN, ks, 0); SB(); \
            AMM(qa[1], AO[1], XO); WRD(AN, ks, 1); SB(); \
            AMM(qa[2], AO[2], XO); WRD(AN, ks, 2); SB(); \
            AMM(qa[3], AO[3], XO); WRD(AN, ks, 3); SB(); } while (0)
        asm volatile("s_nop 1" : "+v"(qa[0]), "+v"(qa[1]), "+v"(qa[2]), "+v"(qa[3]), "+v"(xA), "+v"(aA[0]), "+v"(aA[1]), "+v"(aA[2]), "+v"(aA[3]),
                     "+v"(xB), "+v"(aB[0]), "+v"(aB[1]), "+v"(aB[2]), "+v"(aB[3]));
        AWAITP(have, PL_OFF, 1, 1); ST(1); STC(43);
        __builtin_amdgcn_s_waitcnt(0xC07F);
        for (int j = 0; j < 10; ++j) {
            if (have <= (unsigned)(2 * j + 1)) AWAITP(have, PL_OFF, 2 * j + 1, 1);
            asm volatile("" ::: "memory");
            const LAS char* sb = lds + (j & 3) * SLOT;
            QSTEP(xA, aA, xC, aC, 0); QSTEP(xB, aB, xD, aD, 1); QSTEP(xC, aC, xA, aA, 2);
            const u32x4 nfv = lds_peek4(lds + PL_OFF); SB();
            QSTEP(xD, aD, xB, aB, 3);
            lds_post(pcw, j + 1);
            have = min4(nfv);
        }
        AMM(qa[0], aA[0], xA); AMM(qa[1], aA[1], xA); AMM(qa[2], aA[2], xA); AMM(qa[3], aA[3], xA);
        AMM(qa[0], aB[0], xB); AMM(qa[1], aB[1], xB); AMM(qa[2], aB[2], xB); AMM(qa[3], aB[3], xB);
        asm volatile("s_nop 15" : "+v"(qa[0]), "+v"(qa[1]), "+v"(qa[2]), "+v"(qa[3]));
        ST(2); STC(44);
#pragma unroll
        for (int db = 0; db < 4; ++db) {
#pragma unroll
            for (int i = 0; i < 16; ++i) qa[db][i] *= QSCALE;
            qf[2 * db] = pack8(qa[db], 0); qf[2 * db + 1] = pack8(qa[db], 8);
        }
    }
    unsigned tok = 1;
    if (g == 0) {
        while (__any(tokv == tok)) ++tok;
        if (l == 0) __hip_atomic_store((gu32*)(ws + WS_M + wg * 128), tok, RLX_AGENT);
    }
    ST(3);
    f32x16 o[4] = {}; f32x16 negm; float m, lsum = 0.f;
    {
        const unsigned kb0 = 1024 * (r >> 2) + 64 * (r & 3) + 16 * ((hh) ^ ((r >> 2) & 3));
        const unsigned kb1 = 1024 * (r >> 2) + 64 * (r & 3) + 16 * ((2 + hh) ^ ((r >> 2) & 3));
        const int blk = (l >> 4) & 1, q = (l & 15) >> 2, p = l & 3;
        const unsigned vb0 = 16384 + 1024 * hh + 64 * q + 16 * ((2 * blk + (p >> 1)) ^ (hh)) + 8 * (p & 1);
        const unsigned vb1 = 16384 + 2048 + 1024 * hh + 64 * q + 16 * ((2 * blk + (p >> 1)) ^ (2 + hh)) + 8 * (p & 1);
        u32x4 pA0, pA1, pB0 = {}, pB1 = {}; bf16x8 kf[8], vf0[4] = {}, vf1[4] = {};
        f32x16 sA = {}, sB;
        {
#pragma unroll
            for (int ks = 0; ks < 8; ++ks) sA = MFMA(*(const LAS bf16x8*)(lds + K0_OFF + ((ks & 1) ? kb1 : kb0) + 256 * (ks >> 1)), qf[ks], sA);
            m = swap32max(max16(sA));
#pragma unroll
            for (int i = 0; i < 16; ++i) { sA[i] -= m; negm[i] = -m; }
        }
#define KRD(ks) kf[ks] = *(const LAS bf16x8*)(sb + (((ks) & 1) ? kb1 : kb0) + 256 * ((ks) >> 1))
#define VRD(V, off) V = cat(trd(sb + vb0 + (off)), trd(sb + vb1 + (off)))
#define SMX(C, P0, P1, i) do { e_[(i) & 3] = EX(C[i]); if ((i) > 0) ps += e_[((i) - 1) & 3]; \
            if (((i) & 1) == 0 && (i) > 0) { const unsigned w_ = cvtpk(e_[((i) - 2) & 3], e_[((i) - 1) & 3]); if ((i) <= 8) { P0[(((i) - 2) >> 1) & 3] = w_; PIN(P0); } else { P1[(((i) - 2) >> 1) & 3] = w_; PIN(P1); } } \
            PIN(e_[(i) & 3]); PIN(ps); } while (0)
#define FIXUP(C, N, P0, P1) do { if (__builtin_expect(__any(!(ps <= PSUM_MAX)), 0)) { \
            asm volatile("s_nop 15" : "+v"(o[0]), "+v"(o[1]), "+v"(o[2]), "+v"(o[3]), "+v"(N)); \
            const float delta_ = fmaxf(swap32max(max16(C)), 0.f), f_ = EX(-delta_); m += delta_; lsum *= f_; ps = 0.f; \
            _Pragma("unroll") for (int i = 0; i < 16; ++i) { C[i] = EX(C[i] - delta_); ps += C[i]; N[i] -= delta_; negm[i] = -m; } \
            P0 = __builtin_bit_cast(u32x4, pack8(C, 0)); P1 = __builtin_bit_cast(u32x4, pack8(C, 8)); \
            _Pragma("unroll") for (int db = 0; db < 4; ++db) _Pragma("unroll") for (int i = 0; i < 16; ++i) o[db][i] *= f_; \
            asm volatile("s_nop 1" : "+v"(o[0]), "+v"(o[1]), "+v"(o[2]), "+v"(o[3]), "+v"(negm), "+v"(P0), "+v"(P1)); } } while (0)
#define STEP(C, N, P0, P1, Q0, Q1, MID, LATE) do { float ps = 0.f, e_[4]; \
            AMM(o[0], vf0[0], Q0); SB(); KRD(0); KRD(1); SB(); \
            AMM(o[1], vf0[1], Q0); SB(); KRD(2); KRD(3); SB(); \
            AMM(o[2], vf0[2], Q0); SB(); KRD(4); KRD(5); asm volatile("" : "+v"(C)); SMX(C, P0, P1, 0); SB(); \
            AMM(o[3], vf0[3], Q0); SB(); KRD(6); KRD(7); SMX(C, P0, P1, 1); SB(); \
            MID; \
            AMM(o[0], vf1[0], Q1); SB(); VRD(vf0[0], 0); SMX(C, P0, P1, 2); SB(); \
            AMM(o[1], vf1[1], Q1); SB(); VRD(vf0[1], 256); SMX(C, P0, P1, 3); SB(); \
            AMM(o[2], vf1[2], Q1); SB(); VRD(vf0[2], 512); SMX(C, P0, P1, 4); SB(); \
            AMM(o[3], vf1[3], Q1); SB(); VRD(vf0[3], 768); SMX(C, P0, P1, 5); SB(); \
            AMM0(N, kf[0], qf[0], negm); SB(); VRD(vf1[0], 4096); SMX(C, P0, P1, 6); SB(); \
            AMM(N, kf[1], qf[1]); SB(); VRD(vf1[1], 4096 + 256); SMX(C, P0, P1, 7); SB(); \
            AMM(N, kf[2], qf[2]); SB(); VRD(vf1[2], 4096 + 512); SMX(C, P0, P1, 8); SB(); \
            AMM(N, kf[3], qf[3]); SB(); VRD(vf1[3], 4096 + 768); SMX(C, P0, P1, 9); SB(); \
            AMM(N, kf[4], qf[4]); SB(); LATE; SMX(C, P0, P1, 10); SMX(C, P0, P1, 11); SB(); \
            AMM(N, kf[5], qf[5]); SB(); SMX(C, P0, P1, 12); SMX(C, P0, P1, 13); SB(); \
            AMM(N, kf[6], qf[6]); SB(); SMX(C, P0, P1, 14); SMX(C, P0, P1, 15); SB(); \
            AMM(N, kf[7], qf[7]); SB(); ps += e_[3]; P1[3] = cvtpk(e_[2], e_[3]); PIN(P1); PIN(ps); SB(); \
            FIXUP(C, N, P0, P1); \
            lsum += ps; } while (0)
        asm volatile("s_nop 1" : "+v"(o[0]), "+v"(o[1]), "+v"(o[2]), "+v"(o[3]), "+v"(negm), "+v"(pB0), "+v"(pB1), "+v"(vf0[0]), "+v"(vf0[1]), "+v"(vf0[2]), "+v"(vf0[3]),
                     "+v"(vf1[0]), "+v"(vf1[1]), "+v"(vf1[2]), "+v"(vf1[3]));
        ST(4); STC(41);
        __builtin_amdgcn_s_waitcnt(0xC07F);
        for (int u = 0; u < 15; ++u) {
            if (have <= (unsigned)(20 + 2 * u)) AWAITP(have, PL_OFF, 20 + 2 * u, 1);
            asm volatile("" ::: "memory");
            u32x4 nfv;
            { const LAS char* sb = lds + ((u + 2) & 3) * SLOT;        STEP(sA, sB, pA0, pA1, pB0, pB1, if (have <= (unsigned)(21 + 2 * u)) AWAITP(have, PL_OFF, 21 + 2 * u, 1), (void)0); }
            { const LAS char* sb = lds + ((u + 2) & 3) * SLOT + 8192; STEP(sB, sA, pB0, pB1, pA0, pA1, (void)0,
                  { lds_post(pcw, 11 + u); nfv = lds_peek4(lds + PL_OFF); }); }
            have = min4(nfv);
        }
        ST(5); STC(42);
        {
            const LAS char* sb = lds + 1 * SLOT;
            if (have <= 50u) AWAITP(have, PL_OFF, 50, 1);
            { float ps = 0.f, e_[4];
              AMM(o[0], vf0[0], pB0); SB(); KRD(0); KRD(1); SB();
              AMM(o[1], vf0[1], pB0); SB(); KRD(2); KRD(3); SB();
              AMM(o[2], vf0[2], pB0); SB(); KRD(4); KRD(5); asm volatile("" : "+v"(sA)); SMX(sA, pA0, pA1, 0); SB();
              AMM(o[3], vf0[3], pB0); SB(); KRD(6); KRD(7); SMX(sA, pA0, pA1, 1); SB();
              AMM(o[0], vf1[0], pB1); SB(); SMX(sA, pA0, pA1, 2); SB();
              AMM(o[1], vf1[1], pB1); SB(); SMX(sA, pA0, pA1, 3); SB();
              AMM(o[2], vf1[2], pB1); SB(); SMX(sA, pA0, pA1, 4); SB();
              AMM(o[3], vf1[3], pB1); SB(); SMX(sA, pA0, pA1, 5); SB();
              AMM0(sB, kf[0], qf[0], negm); SB(); SMX(sA, pA0, pA1, 6); SB();
              AMM(sB, kf[1], qf[1]); SB(); SMX(sA, pA0, pA1, 7); SB();
              AMM(sB, kf[2], qf[2]); SB(); SMX(sA, pA0, pA1, 8); SB();
              AMM(sB, kf[3], qf[3]); SB(); SMX(sA, pA0, pA1, 9); SB();
              AMM(sB, kf[4], qf[4]); SB(); SMX(sA, pA0, pA1, 10); SMX(sA, pA0, pA1, 11); SB();
              AMM(sB, kf[5], qf[5]); SB(); SMX(sA, pA0, pA1, 12); SMX(sA, pA0, pA1, 13); SB();
              AMM(sB, kf[6], qf[6]); SB(); SMX(sA, pA0, pA1, 14); SMX(sA, pA0, pA1, 15); SB();
              AMM(sB, kf[7], qf[7]); SB(); ps += e_[3]; pA1[3] = cvtpk(e_[2], e_[3]); PIN(pA1); PIN(ps); SB();
              FIXUP(sA, sB, pA0, pA1);
              lsum += ps; }
            asm volatile("s_nop 15" : "+v"(sB));
            { const float delta_ = ceilf(fmaxf(swap32max(max16(sB)) - 8.f, 0.f));
              if (__builtin_expect(__any(delta_ > 0.f), 0)) {
                  const float f_ = EX(-delta_); m += delta_; lsum *= f_;
#pragma unroll
                  for (int i = 0; i < 4; ++i) { pA0[i] = cvtpk(__uint_as_float(pA0[i] << 16) * f_, __uint_as_float(pA0[i] & 0xffff0000u) * f_);
                                                pA1[i] = cvtpk(__uint_as_float(pA1[i] << 16) * f_, __uint_as_float(pA1[i] & 0xffff0000u) * f_); }
#pragma unroll
                  for (int i = 0; i < 16; ++i) sB[i] -= delta_;
#pragma unroll
                  for (int db = 0; db < 4; ++db)
#pragma unroll
                      for (int i = 0; i < 16; ++i) o[db][i] *= f_;
              }
              float ps = 0.f;
#pragma unroll
              for (int i = 0; i < 16; ++i) { sB[i] = EX(sB[i]); ps += sB[i]; }
              lsum += ps;
              pB0 = __builtin_bit_cast(u32x4, pack8(sB, 0)); pB1 = __builtin_bit_cast(u32x4, pack8(sB, 8));
              asm volatile("s_nop 1" : "+v"(o[0]), "+v"(o[1]), "+v"(o[2]), "+v"(o[3]), "+v"(pA0), "+v"(pA1), "+v"(pB0), "+v"(pB1), "+v"(lsum)); }
            if (have <= 51u) AWAITP(have, PL_OFF, 51, 1);
            ST(6);
            bf16x8 vg0[4], vg1[4];
            VRD(vf0[0], 0); VRD(vf0[1], 256); VRD(vf0[2], 512); VRD(vf0[3], 768);
            VRD(vf1[0], 4096); VRD(vf1[1], 4096 + 256); VRD(vf1[2], 4096 + 512); VRD(vf1[3], 4096 + 768); SB();
            AMM(o[0], vf0[0], pA0); VRD(vg0[0], 8192); SB(); AMM(o[1], vf0[1], pA0); VRD(vg0[1], 8192 + 256); SB();
            AMM(o[2], vf0[2], pA0); VRD(vg0[2], 8192 + 512); SB(); AMM(o[3], vf0[3], pA0); VRD(vg0[3], 8192 + 768); SB();
            AMM(o[0], vf1[0], pA1); VRD(vg1[0], 12288); SB(); AMM(o[1], vf1[1], pA1); VRD(vg1[1], 12288 + 256); SB();
            AMM(o[2], vf1[2], pA1); VRD(vg1[2], 12288 + 512); SB(); AMM(o[3], vf1[3], pA1); VRD(vg1[3], 12288 + 768); SB();
            AMM(o[0], vg0[0], pB0); AMM(o[1], vg0[1], pB0); AMM(o[2], vg0[2], pB0); AMM(o[3], vg0[3], pB0);
            AMM(o[0], vg1[0], pB1); AMM(o[1], vg1[1], pB1); AMM(o[2], vg1[2], pB1); AMM(o[3], vg1[3], pB1);
            asm volatile("s_nop 15" : "+v"(o[0]), "+v"(o[1]), "+v"(o[2]), "+v"(o[3]));
        }
    }
    {
        ST(7);
        if (g == 0) { VMW(0); lds_post(lds0 + TOK_OFF, tok); }
        lds_post(pcw, 26);
        const float inv = 1.0f / swap32sum(lsum);
        bf16x8 of[8];
#pragma unroll
        for (int db = 0; db < 4; ++db) {
#pragma unroll
            for (int i = 0; i < 16; ++i) o[db][i] *= inv;
            of[2 * db] = pack8(o[db], 0); of[2 * db + 1] = pack8(o[db], 8);
        }
#pragma unroll
        for (int s = 0; s < 8; ++s) __builtin_amdgcn_raw_buffer_store_b128(__builtin_bit_cast(u32x4, of[s]), rWS, l * 16, WS_O + ((wg * 4 + g) * 8 + s) * 1024, 16);
        const int blk = (l >> 4) & 1, q = (l & 15) >> 2, p = l & 3;
        const unsigned ob = 1280 * hh + 64 * q + 16 * (2 * blk + (p >> 1)) + 8 * (p & 1);
        f32x16 acc[5] = {};
        bf16x8 aA[5], aB[5];
        asm volatile("s_nop 1" : "+v"(acc[0]), "+v"(acc[1]), "+v"(acc[2]), "+v"(acc[3]), "+v"(acc[4]), "+v"(of[0]), "+v"(of[1]), "+v"(of[2]), "+v"(of[3]), "+v"(of[4]), "+v"(of[5]), "+v"(of[6]), "+v"(of[7]));
#define ORD(A, s, nb) A[nb] = cat(trd(wb + 5120 * (s) + 256 * (nb)), trd(wb + 5120 * (s) + 2560 + 256 * (nb)))
#define OSTEP(AO, AN, F, sn) do { \
            AMM(acc[0], AO[0], F); if ((sn) < 8) ORD(AN, (sn) & 7, 0); SB(); AMM(acc[1], AO[1], F); if ((sn) < 8) ORD(AN, (sn) & 7, 1); SB(); AMM(acc[2], AO[2], F); if ((sn) < 8) ORD(AN, (sn) & 7, 2); SB(); \
            AMM(acc[3], AO[3], F); if ((sn) < 8) ORD(AN, (sn) & 7, 3); SB(); AMM(acc[4], AO[4], F); if ((sn) < 8) ORD(AN, (sn) & 7, 4); SB(); } while (0)
#define OHEAD(F, off) do { const LAS char* wb = lds + (off) + ob; ORD(aA, 0, 0); ORD(aA, 0, 1); ORD(aA, 0, 2); ORD(aA, 0, 3); ORD(aA, 0, 4); SB(); \
            OSTEP(aA, aB, F[0], 1); OSTEP(aB, aA, F[1], 2); OSTEP(aA, aB, F[2], 3); OSTEP(aB, aA, F[3], 4); OSTEP(aA, aB, F[4], 5); OSTEP(aB, aA, F[5], 6); OSTEP(aA, aB, F[6], 7); OSTEP(aB, aA, F[7], 8); } while (0)
        AWAITP(have, PL_OFF, 52, 1);
        OHEAD(of, 65536);
        VMW(0);
        lds_post(pcw, 27);
        ST(9);
        AWAIT1(SY_OFF, 1);
        ST(10);
        bf16x8 f1[8], f2[8], f3[8];
#define OLOAD(F, cc) do { const unsigned pw_ = (((b << 2) | ((hl + (cc)) & 3)) << 3) | xg; _Pragma("unroll") for (int s = 0; s < 8; ++s) \
            F[s] = __builtin_bit_cast(bf16x8, __builtin_amdgcn_raw_buffer_load_b128(rWS, l * 16, WS_O + ((pw_ * 4 + g) * 8 + s) * 1024, 16)); } while (0)
        OLOAD(f1, 1); OLOAD(f2, 2); OLOAD(f3, 3);
        AWAITP(have, PL_OFF, 54, 1);
        OHEAD(f1, 106496); OHEAD(f2, 0);
        AWAITP(have, PL_OFF, 55, 1);
        OHEAD(f3, 65536);
        asm volatile("s_nop 15" : "+v"(acc[0]), "+v"(acc[1]), "+v"(acc[2]), "+v"(acc[3]), "+v"(acc[4]));
        ST(11);
#pragma unroll
        for (int u = 0; u < 10; ++u) __builtin_amdgcn_raw_buffer_store_b128(__builtin_bit_cast(u32x4, pack8(acc[u >> 1], 8 * (u & 1))), rWS, l * 16, WS_P + (wg * 40 + g * 10 + u) * 1024, 16);
        VMW(0);
        lds_post(pcw, 28);
        ST(12);
        AWAIT1(SY_OFF, 2);
        ST(13);
        REDUCE(g);
        ST(8);
    }
}

extern "C" void kernel_launch(void* const* d_in, const int* in_sizes, int n_in, void* d_out, int out_size, void* d_ws, size_t ws_size, hipStream_t stream) {
    (void)hipFuncSetAttribute((const void*)k_fused, hipFuncAttributeMaxDynamicSharedMemorySize, LDS_BYTES);
    k_fused<<<256, 512, LDS_BYTES, stream>>>((const char*)d_in[0], (const char*)d_in[1], (const char*)d_in[2], (const char*)d_in[3], (const char*)d_in[4], (char*)d_out, (char*)d_ws);
}
```

```cpp
#define POLK df
#define POLV nt
#define WQDF 1
#define WODF 1
#include <hip/hip_runtime.h>
#include <hip/hip_bf16.h>

typedef short bf16x8 __attribute__((ext_vector_type(8)));
typedef short s16x4 __attribute__((ext_vector_type(4)));
typedef float f32x16 __attribute__((ext_vector_type(16)));
typedef float f32x4 __attribute__((ext_vector_type(4)));
typedef unsigned u32x4 __attribute__((ext_vector_type(4)));
typedef unsigned u32x2 __attribute__((ext_vector_type(2)));
typedef __bf16 bf16x2_t __attribute__((ext_vector_type(2)));
typedef float f32x2 __attribute__((ext_vector_type(2)));
#define LAS __attribute__((address_space(3)))
#define DI __device__ __forceinline__
#define MFMA(a, b, c) __builtin_amdgcn_mfma_f32_32x32x16_bf16((a), (b), (c), 0, 0, 0)

constexpr int SLOT = 32768;
constexpr int LDS_BYTES = 163840;
constexpr float QSCALE = 0.08838834764831845f * 1.4426950408889634f;
constexpr float PSUM_MAX = 4096.0f;
constexpr unsigned WS_O = 0;
constexpr unsigned WS_P = 8u << 20;
constexpr unsigned WS_M = 20u << 20;
constexpr unsigned WS_R = WS_M + 32768;

DI unsigned cvtpk(float lo, float hi) { f32x2 v = {lo, hi}; bf16x2_t b = __builtin_convertvector(v, bf16x2_t); return __builtin_bit_cast(unsigned, b); }
DI bf16x8 pack8(const f32x16& x, int base) {
    u32x4 w = {cvtpk(x[base + 0], x[base + 1]), cvtpk(x[base + 2], x[base + 3]), cvtpk(x[base + 4], x[base + 5]), cvtpk(x[base + 6], x[base + 7])};
    return __builtin_bit_cast(bf16x8, w);
}
DI s16x4 trd(const LAS char* p) { return __builtin_bit_cast(s16x4, __builtin_amdgcn_ds_read_tr16_b64_v4i16((LAS s16x4*)p)); }
DI bf16x8 cat(s16x4 a, s16x4 b) { return __builtin_shufflevector(a, b, 0, 1, 2, 3, 4, 5, 6, 7); }
DI float swap32max(float v) { auto r = __builtin_amdgcn_permlane32_swap(__float_as_uint(v), __float_as_uint(v), false, false); return fmaxf(__uint_as_float(r[0]), __uint_as_float(r[1])); }
DI float swap32sum(float v) { auto r = __builtin_amdgcn_permlane32_swap(__float_as_uint(v), __float_as_uint(v), false, false); return __uint_as_float(r[0]) + __uint_as_float(r[1]); }
DI float max16(const f32x16& s) {
    float a = fmaxf(fmaxf(s[0], s[1]), s[2]), b = fmaxf(fmaxf(s[3], s[4]), s[5]);
    a = fmaxf(fmaxf(a, s[6]), s[7]); b = fmaxf(fmaxf(b, s[8]), s[9]); a = fmaxf(fmaxf(a, s[10]), s[11]); b = fmaxf(fmaxf(b, s[12]), s[13]);
    return fmaxf(fmaxf(fmaxf(a, s[14]), s[15]), b);
}
DI void dma_nt(__amdgpu_buffer_rsrc_t r, unsigned voff, unsigned soff, unsigned ldsaddr) {
    asm volatile("s_mov_b32 m0, %3\n\ts_nop 0\n\tbuffer_load_dwordx4 %0, %1, %2 offen nt lds" :: "v"(voff), "s"(r), "s"(soff), "s"(ldsaddr) : "memory");
}
DI void dma_df(__amdgpu_buffer_rsrc_t r, unsigned voff, unsigned soff, unsigned ldsaddr) {
    asm volatile("s_mov_b32 m0, %3\n\ts_nop 0\n\tbuffer_load_dwordx4 %0, %1, %2 offen lds" :: "v"(voff), "s"(r), "s"(soff), "s"(ldsaddr) : "memory");
}
DI u32x4 ld_df(__amdgpu_buffer_rsrc_t r, unsigned voff, unsigned soff) {
    u32x4 v; asm volatile("buffer_load_dwordx4 %0, %1, %2, %3 offen" : "=v"(v) : "v"(voff), "s"(r), "s"(soff) : "memory"); return v;
}
DI u32x4 ld_nt(__amdgpu_buffer_rsrc_t r, unsigned voff, unsigned soff) {
    u32x4 v; asm volatile("buffer_load_dwordx4 %0, %1, %2, %3 offen nt" : "=v"(v) : "v"(voff), "s"(r), "s"(soff) : "memory"); return v;
}
#define ST(k)
#define STC(k)
#define WAIT_BAR(N) asm volatile("s_waitcnt vmcnt(" #N ") lgkmcnt(0)\n\ts_barrier" ::: "memory")
#define LBAR() asm volatile("s_waitcnt lgkmcnt(0)\n\ts_barrier" ::: "memory")
#define PIN4(a) asm volatile("" : "+v"(a[0]), "+v"(a[1]), "+v"(a[2]), "+v"(a[3]))
#define SB() __builtin_amdgcn_sched_barrier(0)
#define PIN(x) asm volatile("" : "+v"(x))
#define EX(x) __builtin_amdgcn_exp2f(x)
#define AMM(D, A, B) asm volatile("v_mfma_f32_32x32x16_bf16 %0, %1, %2, %0" : "+v"(D) : "v"(A), "v"(B))
#define AMM0(D, A, B, C0) asm volatile("v_mfma_f32_32x32x16_bf16 %0, %1, %2, %3" : "=&v"(D) : "v"(A), "v"(B), "v"(C0))
DI __amdgpu_buffer_rsrc_t mkrsrc(const void* p, unsigned bytes) { return __builtin_amdgcn_make_buffer_rsrc((void*)p, 0, bytes, 0x00020000); }

constexpr int K0_OFF = 131072, CTL_OFF = 147456;
constexpr int PL_OFF = CTL_OFF, PC_OFF = CTL_OFF + 64, SY_OFF = CTL_OFF + 128, TOK_OFF = CTL_OFF + 132;
DI void lds_post(unsigned addr, unsigned v) { asm volatile("s_mov_b64 exec, 1\n\tds_write_b32 %0, %1\n\ts_mov_b64 exec, -1" :: "v"(addr), "v"(v) : "memory"); }
DI u32x4 lds_peek4(const LAS char* p) { return *(const volatile LAS u32x4*)p; }
DI unsigned lds_peek(const LAS char* p) { return __builtin_amdgcn_readfirstlane(*(const volatile LAS unsigned*)p); }
DI unsigned min4(u32x4 v) { return __builtin_amdgcn_readfirstlane(min(min(v[0], v[1]), min(v[2], v[3]))); }
typedef __attribute__((address_space(1))) unsigned gu32;
#define RLX_AGENT __ATOMIC_RELAXED, __HIP_MEMORY_SCOPE_AGENT
constexpr int SPIN_CAP = 1 << 18;

DI void handoff(char* ws, int me, int pw, bool act, int myslot, int pslot, unsigned tok, unsigned pre, unsigned syaddr, unsigned syval) {
    gu32* pm = (gu32*)(ws + WS_M + pw * 128); gu32* pr = (gu32*)(ws + WS_R + pw * 128) + myslot; gu32* mr = (gu32*)(ws + WS_R + me * 128) + pslot;
    unsigned last = pre;
    if (act) __hip_atomic_store(pr, pre, RLX_AGENT);
    for (int sp = 0; sp < SPIN_CAP; ++sp) {
        unsigned rj = tok;
        if (act) rj = __hip_atomic_load(mr, RLX_AGENT);
        if (__all(rj == tok)) break;
        if ((sp & 3) == 3 && act) { const unsigned mj = __hip_atomic_load(pm, RLX_AGENT); if (mj != last) { __hip_atomic_store(pr, mj, RLX_AGENT); last = mj; } }
        __builtin_amdgcn_s_sleep(1);
    }
    asm volatile("" ::: "memory");
    lds_post(syaddr, syval);
    if (act) { const unsigned mj = __hip_atomic_load(pm, RLX_AGENT); if (mj != last) __hip_atomic_store(pr, mj, RLX_AGENT); }
}

__global__ __launch_bounds__(512, 2) void k_fused(const char* __restrict__ x, const char* __restrict__ K, const char* __restrict__ V,
                                                  const char* __restrict__ wq, const char* __restrict__ wo, char* __restrict__ out, char* __restrict__ ws) {
    extern __shared__ __attribute__((aligned(16))) char lds_[];
    LAS char* lds = (LAS char*)lds_;
    const unsigned lds0 = (unsigned)(size_t)lds;
    const int wg = blockIdx.x, xg = wg & 7, idx = wg >> 3, b = idx >> 2, hl = idx & 3, h = xg * 4 + hl;
    const int tid = threadIdx.x, l = tid & 63, w = __builtin_amdgcn_readfirstlane(tid >> 6), g = w & 3, c = w >> 2;
    const int r = l & 31, hh = l >> 5;
    const size_t bh = (size_t)(b * 32 + h);
    const __amdgpu_buffer_rsrc_t rWS = mkrsrc(ws, 20u << 20);
    ST(0);
    if (tid < 40) *(LAS unsigned*)(lds + CTL_OFF + tid * 4) = 0u;
#define AWAITP(have, off, n, SL) do { int sp_ = 0; while ((have = min4(lds_peek4(lds + (off)))) <= (unsigned)(n) && ++sp_ < SPIN_CAP) __builtin_amdgcn_s_sleep(SL); asm volatile("" ::: "memory"); } while (0)
#define AWAIT1(off, n) do { int sp_ = 0; while (lds_peek(lds + (off)) < (unsigned)(n) && ++sp_ < SPIN_CAP) __builtin_amdgcn_s_sleep(1); asm volatile("" ::: "memory"); } while (0)
#define VMW(N) asm volatile("s_waitcnt vmcnt(" #N ")" ::: "memory")
#define REDUCE(un) do { const int ui_ = 5 * xg + (un), ug_ = ui_ / 10, uu_ = ui_ % 10; u32x4 pv_[8]; \
        _Pragma("unroll") for (int j = 0; j < 8; ++j) pv_[j] = __builtin_amdgcn_raw_buffer_load_b128(rWS, l * 16, WS_P + ((idx * 8 + j) * 40 + ui_) * 1024, 16); \
        float a_[8] = {}; \
        _Pragma("unroll") for (int j = 0; j < 8; ++j) _Pragma("unroll") for (int e = 0; e < 4; ++e) { a_[2 * e] += __uint_as_float(pv_[j][e] << 16); a_[2 * e + 1] += __uint_as_float(pv_[j][e] & 0xffff0000u); } \
        char* o_ = out + ((size_t)(b * 128 + 32 * ug_ + r) * 640 + 160 * hl + 16 * uu_ + 4 * hh) * 2; \
        *(u32x2*)o_ = (u32x2){cvtpk(a_[0], a_[1]), cvtpk(a_[2], a_[3])}; *(u32x2*)(o_ + 16) = (u32x2){cvtpk(a_[4], a_[5]), cvtpk(a_[6], a_[7])}; } while (0)
    if (c == 1) {
        const __amdgpu_buffer_rsrc_t rK = mkrsrc(K + bh * 262144, 262144), rV = mkrsrc(V + bh * 262144, 262144);
        const __amdgpu_buffer_rsrc_t rX = mkrsrc(x + (size_t)b * 163840, 163840), rW = mkrsrc(wq + h * 256, 640 * 8192 - h * 256);
        const __amdgpu_buffer_rsrc_t rO = mkrsrc(wo + (size_t)xg * 512 * 1280 + hl * 320, 512 * 1280 - hl * 320);
#define GRPM 0
        const bool grpA = GRPM == 0 ? (((b ^ h) & 1) != 0) : (GRPM == 1);
        const unsigned lrow = (l >> 2) & 7, lsub = (l >> 5) * 64, l4 = (l >> 4) & 1, l3 = l & 3, rq = (l >> 2) & 3, csub = (l >> 4) * 64;
        const unsigned vk = (4 * g + rq) * 256 + csub + ((l3 ^ g) * 16), vw = (4 * g + rq) * 8192 + csub + ((l3 ^ g) * 16);
        const unsigned vx0 = (32 * g + lrow) * 1280 + lsub + ((l3 ^ l4) * 16), vx1 = (32 * g + 8 + lrow) * 1280 + lsub + ((l3 ^ (2 + l4)) * 16);
        const unsigned dk = lds0 + g * 1024, dx = lds0 + 4 * g * 1024;
#define XHALF(sl) do { const unsigned d_ = dx + (sl) * SLOT, sx_ = jr * 128; \
        dma_df(rX, vx0, sx_, d_); dma_df(rX, vx1, sx_, d_ + 1024); dma_df(rX, vx0, sx_ + 16 * 1280, d_ + 2048); dma_df(rX, vx1, sx_ + 16 * 1280, d_ + 3072); } while (0)
#define DMAWQ dma_df
#define WHALF(sl) do { const unsigned e_ = dk + (sl) * SLOT + 16384, sq_ = jr * (64 * 8192); \
        DMAWQ(rW, vw, sq_, e_); DMAWQ(rW, vw, sq_ + 16 * 8192, e_ + 4096); DMAWQ(rW, vw, sq_ + 32 * 8192, e_ + 8192); DMAWQ(rW, vw, sq_ + 48 * 8192, e_ + 12288); } while (0)
#define CHUNK(sl) do { XHALF(sl); WHALF(sl); jr = jr == 9 ? 0 : jr + 1; } while (0)
#define POLW nt
#define CAT2(a, b) a##b
#define CAT(a, b) CAT2(a, b)
#define LD_rK CAT(ld_, POLK)
#define LD_rV CAT(ld_, POLV)
#define DMA_rK CAT(dma_, POLK)
#define DMA_rV CAT(dma_, POLV)
#define PARK4(P, R, off) do { P[0] = LD_##R(R, vk, (off)); P[1] = LD_##R(R, vk, (off) + 4096); P[2] = LD_##R(R, vk, (off) + 8192); P[3] = LD_##R(R, vk, (off) + 12288); } while (0)
#define UNPARK4(P, off) do { PIN4(P); LAS char* d_ = lds + (off) + g * 1024 + l * 16; *(LAS u32x4*)(d_) = P[0]; *(LAS u32x4*)(d_ + 4096) = P[1]; *(LAS u32x4*)(d_ + 8192) = P[2]; *(LAS u32x4*)(d_ + 12288) = P[3]; } while (0)
#define TDMA4(R, off, dst) do { const unsigned d_ = dk + (dst); DMA_##R(R, vk, (off), d_); DMA_##R(R, vk, (off) + 4096, d_ + 4096); DMA_##R(R, vk, (off) + 8192, d_ + 8192); DMA_##R(R, vk, (off) + 12288, d_ + 12288); } while (0)
        int jr = (b + (hl * 5 + 1) / 2) % 10;
        const unsigned plw = lds0 + PL_OFF + 4 * g; unsigned freed = 0; int pub = 0;
#define PUB(cnt) do { if ((int)(cnt) > pub) { pub = (cnt); lds_post(plw, (unsigned)pub); } } while (0)
#define NEED_SLOT(n) do { const int fi_ = ((n) >> 1) - 4; \
            if (fi_ >= 0 && freed <= (unsigned)fi_) { freed = min4(lds_peek4(lds + PC_OFF)); if (freed <= (unsigned)fi_) { \
                VMW(12); PUB((n) - 3); VMW(8); PUB((n) - 2); VMW(4); PUB((n) - 1); VMW(0); PUB(n); \
                AWAITP(freed, PC_OFF, fi_, 2); } } } while (0)
        dma_nt(rK, vk, 0, dk + K0_OFF); dma_nt(rK, vk, 4096, dk + K0_OFF + 4096);
        CHUNK(0); CHUNK(1);
        LBAR();
        int n0 = 20;
        {
            u32x4 k0[4], v0[4], k1[4], v1[4], k2[4], v2[4], k3[4], v3[4];
            if (grpA) {
                PARK4(k0, rK, 8192); VMW(12); PUB(2); PARK4(v0, rV, 0); VMW(8); PUB(4);
                PARK4(k1, rK, 16384 + 8192); PARK4(v1, rV, 16384);
                PARK4(k2, rK, 2 * 16384 + 8192); PARK4(v2, rV, 2 * 16384); PARK4(k3, rK, 3 * 16384 + 8192); PARK4(v3, rV, 3 * 16384);
            } else { VMW(8); PUB(2); }
            for (int j = 2; j < 10; ++j) {
                NEED_SLOT(2 * j);
                XHALF(j & 3); VMW(12); PUB(2 * j - 2);
                WHALF(j & 3); jr = jr == 9 ? 0 : jr + 1; VMW(12); PUB(2 * j - 1);
            }
            if (grpA) {
                VMW(12); PUB(17); VMW(8); PUB(18); VMW(4); PUB(19); VMW(0); PUB(20);
                AWAITP(freed, PC_OFF, 6, 2); UNPARK4(k0, 2 * SLOT); PUB(21); UNPARK4(v0, 2 * SLOT + 16384); PUB(22);
                AWAITP(freed, PC_OFF, 7, 2); UNPARK4(k1, 3 * SLOT); PUB(23); UNPARK4(v1, 3 * SLOT + 16384); PUB(24);
                AWAITP(freed, PC_OFF, 8, 2); UNPARK4(k2, 0); PUB(25); UNPARK4(v2, 16384); PUB(26);
                AWAITP(freed, PC_OFF, 9, 2); UNPARK4(k3, SLOT); PUB(27); UNPARK4(v3, SLOT + 16384); PUB(28);
                n0 = 28;
            }
        }
        u32x4 w0[10], w1[10], w2[10], w3[10]; unsigned vo[5];
#pragma unroll
        for (int pi = 0; pi < 5; ++pi) { const unsigned bi = 4 * pi + (l >> 4), qd = bi / 5, cb = bi - 5 * qd; vo[pi] = (32 * g + 4 * qd + rq) * 1280 + cb * 64 + l3 * 16; }
#define LDWO ld_df
#define PARKW(W, cc) do { const unsigned so_ = ((hl + (cc)) & 3) * (128 * 1280); _Pragma("unroll") for (int i = 0; i < 10; ++i) W[i] = LDWO(rO, vo[i % 5], so_ + (i / 5) * (16 * 1280)); } while (0)
#define UNPARKW(W, off) do { asm volatile("" : "+v"(W[0]), "+v"(W[1]), "+v"(W[2]), "+v"(W[3]), "+v"(W[4]), "+v"(W[5]), "+v"(W[6]), "+v"(W[7]), "+v"(W[8]), "+v"(W[9])); \
            LAS char* d_ = lds + (off) + g * 10240 + l * 16; _Pragma("unroll") for (int i = 0; i < 10; ++i) *(LAS u32x4*)(d_ + i * 1024) = W[i]; } while (0)
#define KVW VMW(8); PUB(n - 1)
#define KVRANGE(na, nb) for (int n = (na); n < (nb); ++n) { const int u = (n - 20) >> 1, sl = (n >> 1) & 3; \
            if ((n & 1) == 0) { NEED_SLOT(n); TDMA4(rK, u * 16384 + 8192, sl * SLOT); } else TDMA4(rV, u * 16384, sl * SLOT + 16384); \
            KVW; }
        KVRANGE(n0, 32); PARKW(w0, 0); KVRANGE(32, 36); PARKW(w1, 1); KVRANGE(36, 40); PARKW(w2, 2); KVRANGE(40, 44); PARKW(w3, 3); KVRANGE(44, 52);
        VMW(12); PUB(49); VMW(8); PUB(50); VMW(4); PUB(51); VMW(0); PUB(52);
        AWAITP(freed, PC_OFF, 23, 1); UNPARKW(w0, 65536); PUB(53); UNPARKW(w1, 106496); PUB(54);
        AWAITP(freed, PC_OFF, 25, 1); UNPARKW(w2, 0); PUB(55);
        if (g == 0) {
            const int p1 = (((b << 2) | (l & 3)) << 3) | xg, p2 = (idx << 3) | (l & 7);
            const unsigned pre1 = __hip_atomic_load((gu32*)(ws + WS_M + p1 * 128), RLX_AGENT), pre2 = __hip_atomic_load((gu32*)(ws + WS_M + p2 * 128), RLX_AGENT);
            AWAITP(freed, PC_OFF, 26, 1);
            const unsigned tok = lds_peek(lds + TOK_OFF);
            handoff(ws, wg, p1, l < 4 && l != hl, hl, l & 3, tok, pre1, lds0 + SY_OFF, 1u);
            UNPARKW(w3, 65536); PUB(56);
            AWAITP(freed, PC_OFF, 27, 1);
            handoff(ws, wg, p2, l < 8 && l != xg, 8 + xg, 8 + (l & 7), tok, pre2, lds0 + SY_OFF, 2u);
            REDUCE(4);
        } else { AWAITP(freed, PC_OFF, 26, 1); UNPARKW(w3, 65536); PUB(56); }
        return;
    }
    LBAR();
    __builtin_amdgcn_s_setprio(3);
    const unsigned pcw = lds0 + PC_OFF + 4 * g; unsigned have = 0;
    unsigned tokv = 0;
    if (g == 0) tokv = __hip_atomic_load(l < 16 ? (gu32*)(ws + WS_R + wg * 128) + l : (gu32*)(ws + WS_M + wg * 128), RLX_AGENT);
    bf16x8 qf[8];
    {
        f32x16 qa[4] = {};
        const int row = 32 * g + r;
        const unsigned xb0 = 1024 * (row >> 3) + 64 * (row & 7) + 16 * ((hh) ^ ((row >> 2) & 3));
        const unsigned xb1 = 1024 * (row >> 3) + 64 * (row & 7) + 16 * ((2 + hh) ^ ((row >> 2) & 3));
        const int blk = (l >> 4) & 1, q = (l & 15) >> 2, p = l & 3, pp = ((p & 1) << 1) | (p >> 1);
        const unsigned wb0 = 16384 + 2048 * hh + 64 * q + 16 * ((2 * blk + (pp >> 1)) ^ (2 * hh)) + 8 * (pp & 1);
        const unsigned wb1 = 16384 + 2048 * hh + 1024 + 64 * q + 16 * ((2 * blk + (pp >> 1)) ^ (2 * hh + 1)) + 8 * (pp & 1);
        bf16x8 xA = {}, aA[4] = {}, xB = {}, aB[4] = {}, xC, aC[4], xD, aD[4];
#define WRD(A, ks, db) A[db] = cat(trd(sb + wb0 + 4096 * (ks) + 256 * (db)), trd(sb + wb1 + 4096 * (ks) + 256 * (db)))
#define QSTEP(XO, AO, XN, AN, ks) do { \
            AMM(qa[0], AO[0], XO); XN = *(const LAS bf16x8*)(sb + (((ks) & 1) ? xb1 : xb0) + 512 * ((ks) >> 1)); WRD(AN, ks, 0); SB(); \
            AMM(qa[1], AO[1], XO); WRD(AN, ks, 1); SB(); \
            AMM(qa[2], AO[2], XO); WRD(AN, ks, 2); SB(); \
            AMM(qa[3], AO[3], XO); WRD(AN, ks, 3); SB(); } while (0)
        asm volatile("s_nop 1" : "+v"(qa[0]), "+v"(qa[1]), "+v"(qa[2]), "+v"(qa[3]), "+v"(xA), "+v"(aA[0]), "+v"(aA[1]), "+v"(aA[2]), "+v"(aA[3]),
                     "+v"(xB), "+v"(aB[0]), "+v"(aB[1]), "+v"(aB[2]), "+v"(aB[3]));
        AWAITP(have, PL_OFF, 1, 1); ST(1); STC(43);
        __builtin_amdgcn_s_waitcnt(0xC07F);
        for (int j = 0; j < 10; ++j) {
            if (have <= (unsigned)(2 * j + 1)) AWAITP(have, PL_OFF, 2 * j + 1, 1);
            asm volatile("" ::: "memory");
            const LAS char* sb = lds + (j & 3) * SLOT;
            QSTEP(xA, aA, xC, aC, 0); QSTEP(xB, aB, xD, aD, 1); QSTEP(xC, aC, xA, aA, 2);
            const u32x4 nfv = lds_peek4(lds + PL_OFF); SB();
            QSTEP(xD, aD, xB, aB, 3);
            lds_post(pcw, j + 1);
            have = min4(nfv);
        }
        AMM(qa[0], aA[0], xA); AMM(qa[1], aA[1], xA); AMM(qa[2], aA[2], xA); AMM(qa[3], aA[3], xA);
        AMM(qa[0], aB[0], xB); AMM(qa[1], aB[1], xB); AMM(qa[2], aB[2], xB); AMM(qa[3], aB[3], xB);
        asm volatile("s_nop 15" : "+v"(qa[0]), "+v"(qa[1]), "+v"(qa[2]), "+v"(qa[3]));
        ST(2); STC(44);
#pragma unroll
        for (int db = 0; db < 4; ++db) {
#pragma unroll
            for (int i = 0; i < 16; ++i) qa[db][i] *= QSCALE;
            qf[2 * db] = pack8(qa[db], 0); qf[2 * db + 1] = pack8(qa[db], 8);
        }
    }
    unsigned tok = 1;
    if (g == 0) {
        while (__any(tokv == tok)) ++tok;
        if (l == 0) __hip_atomic_store((gu32*)(ws + WS_M + wg * 128), tok, RLX_AGENT);
    }
    ST(3);
    f32x16 o[4] = {}; f32x16 negm; float m, lsum = 0.f;
    {
        const unsigned kb0 = 1024 * (r >> 2) + 64 * (r & 3) + 16 * ((hh) ^ ((r >> 2) & 3));
        const unsigned kb1 = 1024 * (r >> 2) + 64 * (r & 3) + 16 * ((2 + hh) ^ ((r >> 2) & 3));
        const int blk = (l >> 4) & 1, q = (l & 15) >> 2, p = l & 3;
        const unsigned vb0 = 16384 + 1024 * hh + 64 * q + 16 * ((2 * blk + (p >> 1)) ^ (hh)) + 8 * (p & 1);
        const unsigned vb1 = 16384 + 2048 + 1024 * hh + 64 * q + 16 * ((2 * blk + (p >> 1)) ^ (2 + hh)) + 8 * (p & 1);
        u32x4 pA0, pA1, pB0 = {}, pB1 = {}; bf16x8 kf[8], vf0[4] = {}, vf1[4] = {};
        f32x16 sA = {}, sB;
        {
#pragma unroll
            for (int ks = 0; ks < 8; ++ks) sA = MFMA(*(const LAS bf16x8*)(lds + K0_OFF + ((ks & 1) ? kb1 : kb0) + 256 * (ks >> 1)), qf[ks], sA);
            m = swap32max(max16(sA));
#pragma unroll
            for (int i = 0; i < 16; ++i) { sA[i] -= m; negm[i] = -m; }
        }
#define KRD(ks) kf[ks] = *(const LAS bf16x8*)(sb + (((ks) & 1) ? kb1 : kb0) + 256 * ((ks) >> 1))
#define VRD(V, off) V = cat(trd(sb + vb0 + (off)), trd(sb + vb1 + (off)))
#define SMX(C, P0, P1, i) do { e_[(i) & 3] = EX(C[i]); if ((i) > 0) ps += e_[((i) - 1) & 3]; \
            if (((i) & 1) == 0 && (i) > 0) { const unsigned w_ = cvtpk(e_[((i) - 2) & 3], e_[((i) - 1) & 3]); if ((i) <= 8) { P0[(((i) - 2) >> 1) & 3] = w_; PIN(P0); } else { P1[(((i) - 2) >> 1) & 3] = w_; PIN(P1); } } \
            PIN(e_[(i) & 3]); PIN(ps); } while (0)
#define FIXUP(C, N, P0, P1) do { if (__builtin_expect(__any(!(ps <= PSUM_MAX)), 0)) { \
            asm volatile("s_nop 15" : "+v"(o[0]), "+v"(o[1]), "+v"(o[2]), "+v"(o[3]), "+v"(N)); \
            const float delta_ = fmaxf(swap32max(max16(C)), 0.f), f_ = EX(-delta_); m += delta_; lsum *= f_; ps = 0.f; \
            _Pragma("unroll") for (int i = 0; i < 16; ++i) { C[i] = EX(C[i] - delta_); ps += C[i]; N[i] -= delta_; negm[i] = -m; } \
            P0 = __builtin_bit_cast(u32x4, pack8(C, 0)); P1 = __builtin_bit_cast(u32x4, pack8(C, 8)); \
            _Pragma("unroll") for (int db = 0; db < 4; ++db) _Pragma("unroll") for (int i = 0; i < 16; ++i) o[db][i] *= f_; \
            asm volatile("s_nop 1" : "+v"(o[0]), "+v"(o[1]), "+v"(o[2]), "+v"(o[3]), "+v"(negm), "+v"(P0), "+v"(P1)); } } while (0)
#define STEP(C, N, P0, P1, Q0, Q1, MID, LATE) do { float ps = 0.f, e_[4]; \
            AMM(o[0], vf0[0], Q0); SB(); KRD(0); KRD(1); SB(); \
            AMM(o[1], vf0[1], Q0); SB(); KRD(2); KRD(3); SB(); \
            AMM(o[2], vf0[2], Q0); SB(); KRD(4); KRD(5); asm volatile("" : "+v"(C)); SMX(C, P0, P1, 0); SB(); \
            AMM(o[3], vf0[3], Q0); SB(); KRD(6); KRD(7); SMX(C, P0, P1, 1); SB(); \
            MID; \
            AMM(o[0], vf1[0], Q1); SB(); VRD(vf0[0], 0); SMX(C, P0, P1, 2); SB(); \
            AMM(o[1], vf1[1], Q1); SB(); VRD(vf0[1], 256); SMX(C, P0, P1, 3); SB(); \
            AMM(o[2], vf1[2], Q1); SB(); VRD(vf0[2], 512); SMX(C, P0, P1, 4); SB(); \
            AMM(o[3], vf1[3], Q1); SB(); VRD(vf0[3], 768); SMX(C, P0, P1, 5); SB(); \
            AMM0(N, kf[0], qf[0], negm); SB(); VRD(vf1[0], 4096); SMX(C, P0, P1, 6); SB(); \
            AMM(N, kf[1], qf[1]); SB(); VRD(vf1[1], 4096 + 256); SMX(C, P0, P1, 7); SB(); \
            AMM(N, kf[2], qf[2]); SB(); VRD(vf1[2], 4096 + 512); SMX(C, P0, P1, 8); SB(); \
            AMM(N, kf[3], qf[3]); SB(); VRD(vf1[3], 4096 + 768); SMX(C, P0, P1, 9); SB(); \
            AMM(N, kf[4], qf[4]); SB(); LATE; SMX(C, P0, P1, 10); SMX(C, P0, P1, 11); SB(); \
            AMM(N, kf[5], qf[5]); SB(); SMX(C, P0, P1, 12); SMX(C, P0, P1, 13); SB(); \
            AMM(N, kf[6], qf[6]); SB(); SMX(C, P0, P1, 14); SMX(C, P0, P1, 15); SB(); \
            AMM(N, kf[7], qf[7]); SB(); ps += e_[3]; P1[3] = cvtpk(e_[2], e_[3]); PIN(P1); PIN(ps); SB(); \
            FIXUP(C, N, P0, P1); \
            lsum += ps; } while (0)
        asm volatile("s_nop 1" : "+v"(o[0]), "+v"(o[1]), "+v"(o[2]), "+v"(o[3]), "+v"(negm), "+v"(pB0), "+v"(pB1), "+v"(vf0[0]), "+v"(vf0[1]), "+v"(vf0[2]), "+v"(vf0[3]),
                     "+v"(vf1[0]), "+v"(vf1[1]), "+v"(vf1[2]), "+v"(vf1[3]));
        ST(4); STC(41);
        __builtin_amdgcn_s_waitcnt(0xC07F);
        for (int u = 0; u < 15; ++u) {
            if (have <= (unsigned)(20 + 2 * u)) AWAITP(have, PL_OFF, 20 + 2 * u, 1);
            asm volatile("" ::: "memory");
            u32x4 nfv;
            { const LAS char* sb = lds + ((u + 2) & 3) * SLOT;        STEP(sA, sB, pA0, pA1, pB0, pB1, if (have <= (unsigned)(21 + 2 * u)) AWAITP(have, PL_OFF, 21 + 2 * u, 1), (void)0); }
            { const LAS char* sb = lds + ((u + 2) & 3) * SLOT + 8192; STEP(sB, sA, pB0, pB1, pA0, pA1, (void)0,
                  { lds_post(pcw, 11 + u); nfv = lds_peek4(lds + PL_OFF); }); }
            have = min4(nfv);
        }
        ST(5); STC(42);
        {
            const LAS char* sb = lds + 1 * SLOT;
            if (have <= 50u) AWAITP(have, PL_OFF, 50, 1);
            { float ps = 0.f, e_[4];
              AMM(o[0], vf0[0], pB0); SB(); KRD(0); KRD(1); SB();
              AMM(o[1], vf0[1], pB0); SB(); KRD(2); KRD(3); SB();
              AMM(o[2], vf0[2], pB0); SB(); KRD(4); KRD(5); asm volatile("" : "+v"(sA)); SMX(sA, pA0, pA1, 0); SB();
              AMM(o[3], vf0[3], pB0); SB(); KRD(6); KRD(7); SMX(sA, pA0, pA1, 1); SB();
              AMM(o[0], vf1[0], pB1); SB(); SMX(sA, pA0, pA1, 2); SB();
              AMM(o[1], vf1[1], pB1); SB(); SMX(sA, pA0, pA1, 3); SB();
              AMM(o[2], vf1[2], pB1); SB(); SMX(sA, pA0, pA1, 4); SB();
              AMM(o[3], vf1[3], pB1); SB(); SMX(sA, pA0, pA1, 5); SB();
              AMM0(sB, kf[0], qf[0], negm); SB(); SMX(sA, pA0, pA1, 6); SB();
              AMM(sB, kf[1], qf[1]); SB(); SMX(sA, pA0, pA1, 7); SB();
              AMM(sB, kf[2], qf[2]); SB(); SMX(sA, pA0, pA1, 8); SB();
              AMM(sB, kf[3], qf[3]); SB(); SMX(sA, pA0, pA1, 9); SB();
              AMM(sB, kf[4], qf[4]); SB(); SMX(sA, pA0, pA1, 10); SMX(sA, pA0, pA1, 11); SB();
              AMM(sB, kf[5], qf[5]); SB(); SMX(sA, pA0, pA1, 12); SMX(sA, pA0, pA1, 13); SB();
              AMM(sB, kf[6], qf[6]); SB(); SMX(sA, pA0, pA1, 14); SMX(sA, pA0, pA1, 15); SB();
              AMM(sB, kf[7], qf[7]); SB(); ps += e_[3]; pA1[3] = cvtpk(e_[2], e_[3]); PIN(pA1); PIN(ps); SB();
              FIXUP(sA, sB, pA0, pA1);
              lsum += ps; }
            asm volatile("s_nop 15" : "+v"(sB));
            { const float delta_ = ceilf(fmaxf(swap32max(max16(sB)) - 8.f, 0.f));
              if (__builtin_expect(__any(delta_ > 0.f), 0)) {
                  const float f_ = EX(-delta_); m += delta_; lsum *= f_;
#pragma unroll
                  for (int i = 0; i < 4; ++i) { pA0[i] = cvtpk(__uint_as_float(pA0[i] << 16) * f_, __uint_as_float(pA0[i] & 0xffff0000u) * f_);
                                                pA1[i] = cvtpk(__uint_as_float(pA1[i] << 16) * f_, __uint_as_float(pA1[i] & 0xffff0000u) * f_); }
#pragma unroll
                  for (int i = 0; i < 16; ++i) sB[i] -= delta_;
#pragma unroll
                  for (int db = 0; db < 4; ++db)
#pragma unroll
                      for (int i = 0; i < 16; ++i) o[db][i] *= f_;
              }
              float ps = 0.f;
#pragma unroll
              for (int i = 0; i < 16; ++i) { sB[i] = EX(sB[i]); ps += sB[i]; }
              lsum += ps;
              pB0 = __builtin_bit_cast(u32x4, pack8(sB, 0)); pB1 = __builtin_bit_cast(u32x4, pack8(sB, 8));
              asm volatile("s_nop 1" : "+v"(o[0]), "+v"(o[1]), "+v"(o[2]), "+v"(o[3]), "+v"(pA0), "+v"(pA1), "+v"(pB0), "+v"(pB1), "+v"(lsum)); }
            if (have <= 51u) AWAITP(have, PL_OFF, 51, 1);
            ST(6);
            bf16x8 vg0[4], vg1[4];
            VRD(vf0[0], 0); VRD(vf0[1], 256); VRD(vf0[2], 512); VRD(vf0[3], 768);
            VRD(vf1[0], 4096); VRD(vf1[1], 4096 + 256); VRD(vf1[2], 4096 + 512); VRD(vf1[3], 4096 + 768); SB();
            AMM(o[0], vf0[0], pA0); VRD(vg0[0], 8192); SB(); AMM(o[1], vf0[1], pA0); VRD(vg0[1], 8192 + 256); SB();
            AMM(o[2], vf0[2], pA0); VRD(vg0[2], 8192 + 512); SB(); AMM(o[3], vf0[3], pA0); VRD(vg0[3], 8192 + 768); SB();
            AMM(o[0], vf1[0], pA1); VRD(vg1[0], 12288); SB(); AMM(o[1], vf1[1], pA1); VRD(vg1[1], 12288 + 256); SB();
            AMM(o[2], vf1[2], pA1); VRD(vg1[2], 12288 + 512); SB(); AMM(o[3], vf1[3], pA1); VRD(vg1[3], 12288 + 768); SB();
            AMM(o[0], vg0[0], pB0); AMM(o[1], vg0[1], pB0); AMM(o[2], vg0[2], pB0); AMM(o[3], vg0[3], pB0);
            AMM(o[0], vg1[0], pB1); AMM(o[1], vg1[1], pB1); AMM(o[2], vg1[2], pB1); AMM(o[3], vg1[3], pB1);
            asm volatile("s_nop 15" : "+v"(o[0]), "+v"(o[1]), "+v"(o[2]), "+v"(o[3]));
        }
    }
    {
        ST(7);
        if (g == 0) { VMW(0); lds_post(lds0 + TOK_OFF, tok); }
        lds_post(pcw, 26);
        const float inv = 1.0f / swap32sum(lsum);
        bf16x8 of[8];
#pragma unroll
        for (int db = 0; db < 4; ++db) {
#pragma unroll
            for (int i = 0; i < 16; ++i) o[db][i] *= inv;
            of[2 * db] = pack8(o[db], 0); of[2 * db + 1] = pack8(o[db], 8);
        }
#pragma unroll
        for (int s = 0; s < 8; ++s) __builtin_amdgcn_raw_buffer_store_b128(__builtin_bit_cast(u32x4, of[s]), rWS, l * 16, WS_O + ((wg * 4 + g) * 8 + s) * 1024, 16);
        const int blk = (l >> 4) & 1, q = (l & 15) >> 2, p = l & 3;
        const unsigned ob = 1280 * hh + 64 * q + 16 * (2 * blk + (p >> 1)) + 8 * (p & 1);
        f32x16 acc[5] = {};
        bf16x8 aA[5], aB[5];
        asm volatile("s_nop 1" : "+v"(acc[0]), "+v"(acc[1]), "+v"(acc[2]), "+v"(acc[3]), "+v"(acc[4]), "+v"(of[0]), "+v"(of[1]), "+v"(of[2]), "+v"(of[3]), "+v"(of[4]), "+v"(of[5]), "+v"(of[6]), "+v"(of[7]));
#define ORD(A, s, nb) A[nb] = cat(trd(wb + 5120 * (s) + 256 * (nb)), trd(wb + 5120 * (s) + 2560 + 256 * (nb)))
#define OSTEP(AO, AN, F, sn) do { \
            AMM(acc[0], AO[0], F); if ((sn) < 8) ORD(AN, (sn) & 7, 0); SB(); AMM(acc[1], AO[1], F); if ((sn) < 8) ORD(AN, (sn) & 7, 1); SB(); AMM(acc[2], AO[2], F); if ((sn) < 8) ORD(AN, (sn) & 7, 2); SB(); \
            AMM(acc[3], AO[3], F); if ((sn) < 8) ORD(AN, (sn) & 7, 3); SB(); AMM(acc[4], AO[4], F); if ((sn) < 8) ORD(AN, (sn) & 7, 4); SB(); } while (0)
#define OHEAD(F, off) do { const LAS char* wb = lds + (off) + ob; ORD(aA, 0, 0); ORD(aA, 0, 1); ORD(aA, 0, 2); ORD(aA, 0, 3); ORD(aA, 0, 4); SB(); \
            OSTEP(aA, aB, F[0], 1); OSTEP(aB, aA, F[1], 2); OSTEP(aA, aB, F[2], 3); OSTEP(aB, aA, F[3], 4); OSTEP(aA, aB, F[4], 5); OSTEP(aB, aA, F[5], 6); OSTEP(aA, aB, F[6], 7); OSTEP(aB, aA, F[7], 8); } while (0)
        AWAITP(have, PL_OFF, 52, 1);
        OHEAD(of, 65536);
        VMW(0);
        lds_post(pcw, 27);
        ST(9);
        AWAIT1(SY_OFF, 1);
        ST(10);
        bf16x8 f1[8], f2[8], f3[8];
#define OLOAD(F, cc) do { const unsigned pw_ = (((b << 2) | ((hl + (cc)) & 3)) << 3) | xg; _Pragma("unroll") for (int s = 0; s < 8; ++s) \
            F[s] = __builtin_bit_cast(bf16x8, __builtin_amdgcn_raw_buffer_load_b128(rWS, l * 16, WS_O + ((pw_ * 4 + g) * 8 + s) * 1024, 16)); } while (0)
        OLOAD(f1, 1); OLOAD(f2, 2); OLOAD(f3, 3);
        AWAITP(have, PL_OFF, 54, 1);
        OHEAD(f1, 106496); OHEAD(f2, 0);
        AWAITP(have, PL_OFF, 55, 1);
        OHEAD(f3, 65536);
        asm volatile("s_nop 15" : "+v"(acc[0]), "+v"(acc[1]), "+v"(acc[2]), "+v"(acc[3]), "+v"(acc[4]));
        ST(11);
#pragma unroll
        for (int u = 0; u < 10; ++u) __builtin_amdgcn_raw_buffer_store_b128(__builtin_bit_cast(u32x4, pack8(acc[u >> 1], 8 * (u & 1))), rWS, l * 16, WS_P + (wg * 40 + g * 10 + u) * 1024, 16);
        VMW(0);
        lds_post(pcw, 28);
        ST(12);
        AWAIT1(SY_OFF, 2);
        ST(13);
        REDUCE(g);
        ST(8);
    }
}

extern "C" void kernel_launch(void* const* d_in, const int* in_sizes, int n_in, void* d_out, int out_size, void* d_ws, size_t ws_size, hipStream_t stream) {
    (void)hipFuncSetAttribute((const void*)k_fused, hipFuncAttributeMaxDynamicSharedMemorySize, LDS_BYTES);
    k_fused<<<256, 512, LDS_BYTES, stream>>>((const char*)d_in[0], (const char*)d_in[1], (const char*)d_in[2], (const char*)d_in[3], (const char*)d_in[4], (char*)d_out, (char*)d_ws);
}
```

```cpp
#include <hip/hip_runtime.h>
#include <hip/hip_bf16.h>

typedef short bf16x8 __attribute__((ext_vector_type(8)));
typedef short s16x4 __attribute__((ext_vector_type(4)));
typedef float f32x16 __attribute__((ext_vector_type(16)));
typedef float f32x4 __attribute__((ext_vector_type(4)));
typedef unsigned u32x4 __attribute__((ext_vector_type(4)));
typedef unsigned u32x2 __attribute__((ext_vector_type(2)));
typedef __bf16 bf16x2_t __attribute__((ext_vector_type(2)));
typedef float f32x2 __attribute__((ext_vector_type(2)));
#define LAS __attribute__((address_space(3)))
#define DI __device__ __forceinline__
#define MFMA(a, b, c) __builtin_amdgcn_mfma_f32_32x32x16_bf16((a), (b), (c), 0, 0, 0)

constexpr int SLOT = 32768;
constexpr int LDS_BYTES = 163840;
constexpr float QSCALE = 0.08838834764831845f * 1.4426950408889634f;
constexpr float PSUM_MAX = 4096.0f;
constexpr unsigned WS_O = 0;
constexpr unsigned WS_P = 8u << 20;
constexpr unsigned WS_M = 20u << 20;
constexpr unsigned WS_R = WS_M + 32768;

DI unsigned cvtpk(float lo, float hi) { f32x2 v = {lo, hi}; bf16x2_t b = __builtin_convertvector(v, bf16x2_t); return __builtin_bit_cast(unsigned, b); }
DI bf16x8 pack8(const f32x16& x, int base) {
    u32x4 w = {cvtpk(x[base + 0], x[base + 1]), cvtpk(x[base + 2], x[base + 3]), cvtpk(x[base + 4], x[base + 5]), cvtpk(x[base + 6], x[base + 7])};
    return __builtin_bit_cast(bf16x8, w);
}
DI s16x4 trd(const LAS char* p) { return __builtin_bit_cast(s16x4, __builtin_amdgcn_ds_read_tr16_b64_v4i16((LAS s16x4*)p)); }
DI bf16x8 cat(s16x4 a, s16x4 b) { return __builtin_shufflevector(a, b, 0, 1, 2, 3, 4, 5, 6, 7); }
DI float swap32max(float v) { auto r = __builtin_amdgcn_permlane32_swap(__float_as_uint(v), __float_as_uint(v), false, false); return fmaxf(__uint_as_float(r[0]), __uint_as_float(r[1])); }
DI float swap32sum(float v) { auto r = __builtin_amdgcn_permlane32_swap(__float_as_uint(v), __float_as_uint(v), false, false); return __uint_as_float(r[0]) + __uint_as_float(r[1]); }
DI float max16(const f32x16& s) {
    float a = fmaxf(fmaxf(s[0], s[1]), s[2]), b = fmaxf(fmaxf(s[3], s[4]), s[5]);
    a = fmaxf(fmaxf(a, s[6]), s[7]); b = fmaxf(fmaxf(b, s[8]), s[9]); a = fmaxf(fmaxf(a, s[10]), s[11]); b = fmaxf(fmaxf(b, s[12]), s[13]);
    return fmaxf(fmaxf(fmaxf(a, s[14]), s[15]), b);
}
DI void dma_nt(__amdgpu_buffer_rsrc_t r, unsigned voff, unsigned soff, unsigned ldsaddr) {
    asm volatile("s_mov_b32 m0, %3\n\ts_nop 0\n\tbuffer_load_dwordx4 %0, %1, %2 offen nt lds" :: "v"(voff), "s"(r), "s"(soff), "s"(ldsaddr) : "memory");
}
DI void dma_df(__amdgpu_buffer_rsrc_t r, unsigned voff, unsigned soff, unsigned ldsaddr) {
    asm volatile("s_mov_b32 m0, %3\n\ts_nop 0\n\tbuffer_load_dwordx4 %0, %1, %2 offen lds" :: "v"(voff), "s"(r), "s"(soff), "s"(ldsaddr) : "memory");
}
DI u32x4 ld_df(__amdgpu_buffer_rsrc_t r, unsigned voff, unsigned soff) {
    u32x4 v; asm volatile("buffer_load_dwordx4 %0, %1, %2, %3 offen" : "=v"(v) : "v"(voff), "s"(r), "s"(soff) : "memory"); return v;
}
#define LBAR() asm volatile("s_waitcnt lgkmcnt(0)\n\ts_barrier" ::: "memory")
#define PIN4(a) asm volatile("" : "+v"(a[0]), "+v"(a[1]), "+v"(a[2]), "+v"(a[3]))
#define SB() __builtin_amdgcn_sched_barrier(0)
#define PIN(x) asm volatile("" : "+v"(x))
#define EX(x) __builtin_amdgcn_exp2f(x)
#define AMM(D, A, B) asm volatile("v_mfma_f32_32x32x16_bf16 %0, %1, %2, %0" : "+v"(D) : "v"(A), "v"(B))
#define AMM0(D, A, B, C0) asm volatile("v_mfma_f32_32x32x16_bf16 %0, %1, %2, %3" : "=&v"(D) : "v"(A), "v"(B), "v"(C0))
DI __amdgpu_buffer_rsrc_t mkrsrc(const void* p, unsigned bytes) { return __builtin_amdgcn_make_buffer_rsrc((void*)p, 0, bytes, 0x00020000); }


constexpr int K0_OFF = 131072, CTL_OFF = 147456;
constexpr int PL_OFF = CTL_OFF, PC_OFF = CTL_OFF + 64, SY_OFF = CTL_OFF + 128, TOK_OFF = CTL_OFF + 132, QX_OFF = CTL_OFF + 144;
DI void lds_post(unsigned addr, unsigned v) { asm volatile("s_mov_b64 exec, 1\n\tds_write_b32 %0, %1\n\ts_mov_b64 exec, -1" :: "v"(addr), "v"(v) : "memory"); }
DI u32x4 lds_peek4(const LAS char* p) { return *(const volatile LAS u32x4*)p; }
DI unsigned lds_peek(const LAS char* p) { return __builtin_amdgcn_readfirstlane(*(const volatile LAS unsigned*)p); }
DI unsigned min4(u32x4 v) { return __builtin_amdgcn_readfirstlane(min(min(v[0], v[1]), min(v[2], v[3]))); }
typedef __attribute__((address_space(1))) unsigned gu32;
#define RLX_AGENT __ATOMIC_RELAXED, __HIP_MEMORY_SCOPE_AGENT
constexpr int SPIN_CAP = 1 << 18;

DI void handoff(char* ws, int me, int pw, bool act, int myslot, int pslot, unsigned tok, unsigned pre, unsigned syaddr, unsigned syval) {
    gu32* pm = (gu32*)(ws + WS_M + pw * 128); gu32* pr = (gu32*)(ws + WS_R + pw * 128) + myslot; gu32* mr = (gu32*)(ws + WS_R + me * 128) + pslot;
    unsigned last = pre;
    if (act) __hip_atomic_store(pr, pre, RLX_AGENT);
    for (int sp = 0; sp < SPIN_CAP; ++sp) {
        unsigned rj = tok;
        if (act) rj = __hip_atomic_load(mr, RLX_AGENT);
        if (__all(rj == tok)) break;
        if ((sp & 3) == 3 && act) { const unsigned mj = __hip_atomic_load(pm, RLX_AGENT); if (mj != last) { __hip_atomic_store(pr, mj, RLX_AGENT); last = mj; } }
        __builtin_amdgcn_s_sleep(1);
    }
    asm volatile("" ::: "memory");
    lds_post(syaddr, syval);
    if (act) { const unsigned mj = __hip_atomic_load(pm, RLX_AGENT); if (mj != last) __hip_atomic_store(pr, mj, RLX_AGENT); }
}

__global__ __launch_bounds__(512, 2) void k_fused(const char* __restrict__ x, const char* __restrict__ K, const char* __restrict__ V,
                                                  const char* __restrict__ wq, const char* __restrict__ wo, char* __restrict__ out, char* __restrict__ ws) {
    extern __shared__ __attribute__((aligned(16))) char lds_[];
    LAS char* lds = (LAS char*)lds_;
    const unsigned lds0 = (unsigned)(size_t)lds;
    const int wg = blockIdx.x, xg = wg & 7, idx = wg >> 3, b = idx >> 2, hl = idx & 3, h = xg * 4 + hl;
    const int tid = threadIdx.x, l = tid & 63, w = __builtin_amdgcn_readfirstlane(tid >> 6), g = w & 3, c = w >> 2;
    const int r = l & 31, hh = l >> 5;
    const size_t bh = (size_t)(b * 32 + h);
    const __amdgpu_buffer_rsrc_t rWS = mkrsrc(ws, 20u << 20);
    if (tid < 40) *(LAS unsigned*)(lds + CTL_OFF + tid * 4) = 0u;
#define AWAITP(have, off, n, SL) do { int sp_ = 0; while ((have = min4(lds_peek4(lds + (off)))) <= (unsigned)(n) && ++sp_ < SPIN_CAP) __builtin_amdgcn_s_sleep(SL); asm volatile("" ::: "memory"); } while (0)
#define AWAIT1(off, n) do { int sp_ = 0; while (lds_peek(lds + (off)) < (unsigned)(n) && ++sp_ < SPIN_CAP) __builtin_amdgcn_s_sleep(1); asm volatile("" ::: "memory"); } while (0)
#define VMW(N) asm volatile("s_waitcnt vmcnt(" #N ")" ::: "memory")
#define REDUCE(un) do { const int ui_ = 5 * xg + (un), ug_ = ui_ / 10, uu_ = ui_ % 10; u32x4 pv_[8]; \
        _Pragma("unroll") for (int j = 0; j < 8; ++j) pv_[j] = __builtin_amdgcn_raw_buffer_load_b128(rWS, l * 16, WS_P + ((idx * 8 + j) * 40 + ui_) * 1024, 16); \
        float a_[8] = {}; \
        _Pragma("unroll") for (int j = 0; j < 8; ++j) _Pragma("unroll") for (int e = 0; e < 4; ++e) { a_[2 * e] += __uint_as_float(pv_[j][e] << 16); a_[2 * e + 1] += __uint_as_float(pv_[j][e] & 0xffff0000u); } \
        char* o_ = out + ((size_t)(b * 128 + 32 * ug_ + r) * 640 + 160 * hl + 16 * uu_ + 4 * hh) * 2; \
        *(u32x2*)o_ = (u32x2){cvtpk(a_[0], a_[1]), cvtpk(a_[2], a_[3])}; *(u32x2*)(o_ + 16) = (u32x2){cvtpk(a_[4], a_[5]), cvtpk(a_[6], a_[7])}; } while (0)
    if (c == 1) {
        const __amdgpu_buffer_rsrc_t rK = mkrsrc(K + bh * 262144, 262144), rV = mkrsrc(V + bh * 262144, 262144);
        const __amdgpu_buffer_rsrc_t rX = mkrsrc(x + (size_t)b * 163840, 163840), rW = mkrsrc(wq + h * 256, 640 * 8192 - h * 256);
        const __amdgpu_buffer_rsrc_t rO = mkrsrc(wo + (size_t)xg * 512 * 1280 + hl * 320, 512 * 1280 - hl * 320);
        const bool grpA = (b % 3) == 1;
        const unsigned lrow = (l >> 2) & 7, lsub = (l >> 5) * 64, l4 = (l >> 4) & 1, l3 = l & 3, rq = (l >> 2) & 3, csub = (l >> 4) * 64;
        const unsigned vk = (4 * g + rq) * 256 + csub + ((l3 ^ g) * 16), vw = (4 * g + rq) * 8192 + csub + ((l3 ^ g) * 16);
        const unsigned vx0 = (32 * g + lrow) * 1280 + lsub + ((l3 ^ l4) * 16), vx1 = (32 * g + 8 + lrow) * 1280 + lsub + ((l3 ^ (2 + l4)) * 16);
        const unsigned dk = lds0 + g * 1024, dx = lds0 + 4 * g * 1024;
#define XHALF(sl) do { const unsigned d_ = dx + (sl) * SLOT, sx_ = jr * 128; \
        dma_df(rX, vx0, sx_, d_); dma_df(rX, vx1, sx_, d_ + 1024); dma_df(rX, vx0, sx_ + 16 * 1280, d_ + 2048); dma_df(rX, vx1, sx_ + 16 * 1280, d_ + 3072); } while (0)
#define WHALF(sl) do { const unsigned e_ = dk + (sl) * SLOT + 16384, sq_ = jr * (64 * 8192); \
        dma_df(rW, vw, sq_, e_); dma_df(rW, vw, sq_ + 16 * 8192, e_ + 4096); dma_df(rW, vw, sq_ + 32 * 8192, e_ + 8192); dma_df(rW, vw, sq_ + 48 * 8192, e_ + 12288); } while (0)
#define CHUNK(sl) do { XHALF(sl); WHALF(sl); jr = jr == 9 ? 0 : jr + 1; } while (0)
#define PARK4(P, R, off) do { P[0] = ld_df(R, vk, (off)); P[1] = ld_df(R, vk, (off) + 4096); P[2] = ld_df(R, vk, (off) + 8192); P[3] = ld_df(R, vk, (off) + 12288); } while (0)
#define UNPARK4(P, off) do { PIN4(P); LAS char* d_ = lds + (off) + g * 1024 + l * 16; *(LAS u32x4*)(d_) = P[0]; *(LAS u32x4*)(d_ + 4096) = P[1]; *(LAS u32x4*)(d_ + 8192) = P[2]; *(LAS u32x4*)(d_ + 12288) = P[3]; } while (0)
#define TDMA4(R, off, dst) do { const unsigned d_ = dk + (dst); dma_df(R, vk, (off), d_); dma_df(R, vk, (off) + 4096, d_ + 4096); dma_df(R, vk, (off) + 8192, d_ + 8192); dma_df(R, vk, (off) + 12288, d_ + 12288); } while (0)
        int jr = (b + (hl * 5 + 1) / 2) % 10;
        const unsigned plw = lds0 + PL_OFF + 4 * g; unsigned freed = 0; int pub = 0;
#define PUB(cnt) do { if ((int)(cnt) > pub) { pub = (cnt); lds_post(plw, (unsigned)pub); } } while (0)
#define NEED_SLOT(n) do { const int fi_ = ((n) >> 1) - 4; \
            if (fi_ >= 0 && freed <= (unsigned)fi_) { freed = min4(lds_peek4(lds + PC_OFF)); if (freed <= (unsigned)fi_) { \
                VMW(12); PUB((n) - 3); VMW(8); PUB((n) - 2); VMW(4); PUB((n) - 1); VMW(0); PUB(n); \
                AWAITP(freed, PC_OFF, fi_, 2); } } } while (0)
        dma_nt(rK, vk, 0, dk + K0_OFF); dma_nt(rK, vk, 4096, dk + K0_OFF + 4096);
        CHUNK(0); CHUNK(1);
        LBAR();
        int n0 = 20;
        {
            u32x4 R0[4], R1[4], R2[4], R3[4], R4[4], R5[4], R6[4], R7[4], R8[4], R9[4], R10[4], R11[4];
            if (grpA) {
                PARK4(R0, rK, 8192); VMW(12); PUB(2); PARK4(R1, rK, 16384 + 8192); VMW(8); PUB(4);
                PARK4(R2, rK, 2 * 16384 + 8192);
                PARK4(R3, rK, 3 * 16384 + 8192);
                PARK4(R4, rK, 4 * 16384 + 8192);
                PARK4(R5, rK, 5 * 16384 + 8192);
                PARK4(R6, rK, 6 * 16384 + 8192);
                PARK4(R7, rK, 7 * 16384 + 8192);
                PARK4(R8, rK, 8 * 16384 + 8192);
                PARK4(R9, rK, 9 * 16384 + 8192);
                PARK4(R10, rK, 10 * 16384 + 8192);
                PARK4(R11, rK, 11 * 16384 + 8192);
            } else { VMW(8); PUB(2); }
#pragma unroll 1
            for (int j = 2; j < 10; ++j) {
                NEED_SLOT(2 * j);
                XHALF(j & 3); VMW(8); PUB(2 * j - 1);
                WHALF(j & 3); jr = jr == 9 ? 0 : jr + 1; VMW(8); PUB(2 * j);
            }
            if (grpA) {
                VMW(12); PUB(17); VMW(8); PUB(18); VMW(4); PUB(19); VMW(0); PUB(20);
                if (freed <= 6u) { freed = min4(lds_peek4(lds + PC_OFF)); if (freed <= 6u) {  AWAITP(freed, PC_OFF, 6, 2); } }
                TDMA4(rV, 0, 81920); UNPARK4(R0, 65536); PUB(21);
                if (freed <= 7u) { freed = min4(lds_peek4(lds + PC_OFF)); if (freed <= 7u) { VMW(0); PUB(22); AWAITP(freed, PC_OFF, 7, 2); } }
                TDMA4(rV, 16384, 114688); VMW(4); PUB(22); UNPARK4(R1, 98304); PUB(23);
                if (freed <= 8u) { freed = min4(lds_peek4(lds + PC_OFF)); if (freed <= 8u) { VMW(0); PUB(24); AWAITP(freed, PC_OFF, 8, 2); } }
                TDMA4(rV, 32768, 16384); VMW(4); PUB(24); UNPARK4(R2, 0); PUB(25);
                if (freed <= 9u) { freed = min4(lds_peek4(lds + PC_OFF)); if (freed <= 9u) { VMW(0); PUB(26); AWAITP(freed, PC_OFF, 9, 2); } }
                TDMA4(rV, 49152, 49152); VMW(4); PUB(26); UNPARK4(R3, 32768); PUB(27);
                if (freed <= 10u) { freed = min4(lds_peek4(lds + PC_OFF)); if (freed <= 10u) { VMW(0); PUB(28); AWAITP(freed, PC_OFF, 10, 2); } }
                TDMA4(rV, 65536, 81920); VMW(4); PUB(28); UNPARK4(R4, 65536); PUB(29);
                if (freed <= 11u) { freed = min4(lds_peek4(lds + PC_OFF)); if (freed <= 11u) { VMW(0); PUB(30); AWAITP(freed, PC_OFF, 11, 2); } }
                TDMA4(rV, 81920, 114688); VMW(4); PUB(30); UNPARK4(R5, 98304); PUB(31);
                if (freed <= 12u) { freed = min4(lds_peek4(lds + PC_OFF)); if (freed <= 12u) { VMW(0); PUB(32); AWAITP(freed, PC_OFF, 12, 2); } }
                TDMA4(rV, 98304, 16384); VMW(4); PUB(32); UNPARK4(R6, 0); PUB(33);
                if (freed <= 13u) { freed = min4(lds_peek4(lds + PC_OFF)); if (freed <= 13u) { VMW(0); PUB(34); AWAITP(freed, PC_OFF, 13, 2); } }
                TDMA4(rV, 114688, 49152); VMW(4); PUB(34); UNPARK4(R7, 32768); PUB(35);
                if (freed <= 14u) { freed = min4(lds_peek4(lds + PC_OFF)); if (freed <= 14u) { VMW(0); PUB(36); AWAITP(freed, PC_OFF, 14, 2); } }
                TDMA4(rV, 131072, 81920); VMW(4); PUB(36); UNPARK4(R8, 65536); PUB(37);
                if (freed <= 15u) { freed = min4(lds_peek4(lds + PC_OFF)); if (freed <= 15u) { VMW(0); PUB(38); AWAITP(freed, PC_OFF, 15, 2); } }
                TDMA4(rV, 147456, 114688); VMW(4); PUB(38); UNPARK4(R9, 98304); PUB(39);
                if (freed <= 16u) { freed = min4(lds_peek4(lds + PC_OFF)); if (freed <= 16u) { VMW(0); PUB(40); AWAITP(freed, PC_OFF, 16, 2); } }
                TDMA4(rV, 163840, 16384); VMW(4); PUB(40); UNPARK4(R10, 0); PUB(41);
                if (freed <= 17u) { freed = min4(lds_peek4(lds + PC_OFF)); if (freed <= 17u) { VMW(0); PUB(42); AWAITP(freed, PC_OFF, 17, 2); } }
                TDMA4(rV, 180224, 49152); VMW(4); PUB(42); UNPARK4(R11, 32768); PUB(43);
                n0 = 44;
            }
        }
        u32x4 w0[10], w1[10], w2[10], w3[10]; unsigned vo[5];
#pragma unroll
        for (int pi = 0; pi < 5; ++pi) { const unsigned bi = 4 * pi + (l >> 4), qd = bi / 5, cb = bi - 5 * qd; vo[pi] = (32 * g + 4 * qd + rq) * 1280 + cb * 64 + l3 * 16; }
#define PARKW(W, cc) do { const unsigned so_ = ((hl + (cc)) & 3) * (128 * 1280); _Pragma("unroll") for (int i = 0; i < 10; ++i) W[i] = ld_df(rO, vo[i % 5], so_ + (i / 5) * (16 * 1280)); } while (0)
#define UNPARKW(W, off) do { asm volatile("" : "+v"(W[0]), "+v"(W[1]), "+v"(W[2]), "+v"(W[3]), "+v"(W[4]), "+v"(W[5]), "+v"(W[6]), "+v"(W[7]), "+v"(W[8]), "+v"(W[9])); \
            LAS char* d_ = lds + (off) + g * 10240 + l * 16; _Pragma("unroll") for (int i = 0; i < 10; ++i) *(LAS u32x4*)(d_ + i * 1024) = W[i]; } while (0)
#define KVW VMW(8); PUB(n - 1)
#define KVRANGE(na, nb) _Pragma("unroll 1") for (int n = (na); n < (nb); ++n) { const int u = (n - 20) >> 1, sl = (n >> 1) & 3; \
            if ((n & 1) == 0) { NEED_SLOT(n); TDMA4(rK, u * 16384 + 8192, sl * SLOT); } else TDMA4(rV, u * 16384, sl * SLOT + 16384); \
            KVW; }
        KVRANGE(n0, 52);
        VMW(12); PUB(49); VMW(8); PUB(50);
        VMW(4); PUB(51);
        PARKW(w0, 0);
        VMW(10); PUB(52);
        PARKW(w1, 1);
        AWAITP(freed, PC_OFF, 23, 1); VMW(10); UNPARKW(w0, 65536); PUB(53);
        PARKW(w2, 2); VMW(10); UNPARKW(w1, 106496); PUB(54);
        PARKW(w3, 3);
        AWAITP(freed, PC_OFF, 25, 1); VMW(10); UNPARKW(w2, 0); PUB(55); VMW(0);
        if (g == 0) {
            const int p1 = (((b << 2) | (l & 3)) << 3) | xg, p2 = (idx << 3) | (l & 7);
            const unsigned pre1 = __hip_atomic_load((gu32*)(ws + WS_M + p1 * 128), RLX_AGENT), pre2 = __hip_atomic_load((gu32*)(ws + WS_M + p2 * 128), RLX_AGENT);
            const unsigned tok = lds_peek(lds + TOK_OFF);
            {
                const bool act = l < 4 && l != hl;
                gu32* pm = (gu32*)(ws + WS_M + p1 * 128); gu32* pr = (gu32*)(ws + WS_R + p1 * 128) + hl; gu32* mr = (gu32*)(ws + WS_R + wg * 128) + (l & 3);
                unsigned last = pre1, seen = 0; bool sent = false, w3done = false;
                for (int sp = 0; sp < SPIN_CAP; ++sp) {
                    if (!sent || !w3done) {
                        freed = min4(lds_peek4(lds + PC_OFF));
                        if (!sent && freed > 26u) { if (act) __hip_atomic_store(pr, last, RLX_AGENT); sent = true; }
                        if (!w3done && freed > 27u) { UNPARKW(w3, 65536); PUB(56); w3done = true; }
                    }
                    if (seen != 15u) {
                        unsigned rj = tok;
                        if (act) rj = __hip_atomic_load(mr, RLX_AGENT);
                        const unsigned now = (unsigned)__ballot(rj == tok) & 15u;
                        if (now != seen) { seen = now; asm volatile("" ::: "memory"); lds_post(lds0 + SY_OFF, seen); }
                    }
                    if (seen == 15u && sent && w3done) break;
                    if (sent && (sp & 3) == 3 && act) { const unsigned mj = __hip_atomic_load(pm, RLX_AGENT); if (mj != last) { __hip_atomic_store(pr, mj, RLX_AGENT); last = mj; } }
                    __builtin_amdgcn_s_sleep(1);
                }
                if (act) { const unsigned mj = __hip_atomic_load(pm, RLX_AGENT); if (mj != last) __hip_atomic_store(pr, mj, RLX_AGENT); }
            }
            AWAITP(freed, PC_OFF, 28, 1);
            handoff(ws, wg, p2, l < 8 && l != xg, 8 + xg, 8 + (l & 7), tok, pre2, lds0 + SY_OFF, 31u);
            REDUCE(4);
        } else { AWAITP(freed, PC_OFF, 27, 1); UNPARKW(w3, 65536); PUB(56); }
        return;
    }
    LBAR();
    __builtin_amdgcn_s_setprio(3);
    const unsigned pcw = lds0 + PC_OFF + 4 * g; unsigned have = 0;
    unsigned tokv = 0;
    if (g == 1 || g == 2) {
        unsigned off;
        if (l < 32) off = WS_O + (((((b << 2) | (l >> 3)) << 3) | xg) * 32 + (l & 7) * 4) * 1024;
        else if (l < 42) off = WS_P + (wg * 40 + (l - 32) * 4) * 1024;
        else off = WS_P + ((idx * 8 + ((l - 42) & 7)) * 40 + 5 * xg + (l < 50 ? 0 : 4)) * 1024;
        if (g == 1) tokv = __builtin_amdgcn_raw_buffer_load_b32(rWS, off, 0, 16);
        else tokv = *(const volatile unsigned*)(out + (size_t)(b * 128 + 2 * l) * 1280 + 320 * hl);
    }
    if (g == 0) tokv = __hip_atomic_load(l < 16 ? (gu32*)(ws + WS_R + wg * 128) + l : (gu32*)(ws + WS_M + wg * 128), RLX_AGENT);
    bf16x8 qf[8];
    {
        f32x16 qa[4] = {};
        const int rg = g >> 1, dh = g & 1, row = 64 * rg + r;
        const unsigned xb0 = 1024 * (row >> 3) + 64 * (row & 7) + 16 * ((hh) ^ ((row >> 2) & 3));
        const unsigned xb1 = 1024 * (row >> 3) + 64 * (row & 7) + 16 * ((2 + hh) ^ ((row >> 2) & 3));
        const int blk = (l >> 4) & 1, q = (l & 15) >> 2, p = l & 3, pp = ((p & 1) << 1) | (p >> 1);
        const unsigned wb0 = 16384 + 512 * dh + 2048 * hh + 64 * q + 16 * ((2 * blk + (pp >> 1)) ^ (2 * hh)) + 8 * (pp & 1);
        const unsigned wb1 = 16384 + 512 * dh + 2048 * hh + 1024 + 64 * q + 16 * ((2 * blk + (pp >> 1)) ^ (2 * hh + 1)) + 8 * (pp & 1);
        bf16x8 xA[2] = {}, aA[2] = {}, xB[2] = {}, aB[2] = {}, xC[2], aC[2], xD[2], aD[2];
#define WRD(A, ks, db) A[db] = cat(trd(sb + wb0 + 4096 * (ks) + 256 * (db)), trd(sb + wb1 + 4096 * (ks) + 256 * (db)))
#define XRD(X, ks, rb) X[rb] = *(const LAS bf16x8*)(sb + (((ks) & 1) ? xb1 : xb0) + 512 * ((ks) >> 1) + 4096 * (rb))
#define QSTEP(XO, AO, XN, AN, ks) do { \
            AMM(qa[0], AO[0], XO[0]); XRD(XN, ks, 0); SB(); \
            AMM(qa[1], AO[1], XO[0]); WRD(AN, ks, 0); SB(); \
            AMM(qa[2], AO[0], XO[1]); WRD(AN, ks, 1); SB(); \
            AMM(qa[3], AO[1], XO[1]); XRD(XN, ks, 1); SB(); } while (0)
        asm volatile("s_nop 1" : "+v"(qa[0]), "+v"(qa[1]), "+v"(qa[2]), "+v"(qa[3]), "+v"(xA[0]), "+v"(xA[1]), "+v"(aA[0]), "+v"(aA[1]), "+v"(xB[0]), "+v"(xB[1]), "+v"(aB[0]), "+v"(aB[1]));
        AWAITP(have, PL_OFF, 1, 1);
        __builtin_amdgcn_s_waitcnt(0xC07F);
        for (int j = 0; j < 10; ++j) {
            if (have <= (unsigned)(2 * j + 1)) AWAITP(have, PL_OFF, 2 * j + 1, 1);
            asm volatile("" ::: "memory");
            const LAS char* sb = lds + (j & 3) * SLOT;
            QSTEP(xA, aA, xC, aC, 0); QSTEP(xB, aB, xD, aD, 1); QSTEP(xC, aC, xA, aA, 2);
            const u32x4 nfv = lds_peek4(lds + PL_OFF); SB();
            QSTEP(xD, aD, xB, aB, 3);
            lds_post(pcw, j + 1);
            have = min4(nfv);
        }
        AMM(qa[0], aA[0], xA[0]); AMM(qa[1], aA[1], xA[0]); AMM(qa[2], aA[0], xA[1]); AMM(qa[3], aA[1], xA[1]);
        AMM(qa[0], aB[0], xB[0]); AMM(qa[1], aB[1], xB[0]); AMM(qa[2], aB[0], xB[1]); AMM(qa[3], aB[1], xB[1]);
        asm volatile("s_nop 15" : "+v"(qa[0]), "+v"(qa[1]), "+v"(qa[2]), "+v"(qa[3]));
        bf16x8 t[8];
#pragma unroll
        for (int i4 = 0; i4 < 4; ++i4) {
#pragma unroll
            for (int i = 0; i < 16; ++i) qa[i4][i] *= QSCALE;
            t[2 * i4] = pack8(qa[i4], 0); t[2 * i4 + 1] = pack8(qa[i4], 8);
        }
        LAS char* mine = lds + (rg ? 155648 : 139264) + dh * 4096 + l * 16; const LAS char* theirs = lds + (rg ? 155648 : 139264) + (dh ^ 1) * 4096 + l * 16;
        if (dh) {
#pragma unroll
            for (int i = 0; i < 4; ++i) { *(LAS bf16x8*)(mine + i * 1024) = t[i]; qf[4 + i] = t[4 + i]; }
            lds_post(lds0 + QX_OFF + 4 * g, 1u); AWAIT1(QX_OFF + 4 * (g ^ 1), 1);
#pragma unroll
            for (int i = 0; i < 4; ++i) qf[i] = *(const LAS bf16x8*)(theirs + i * 1024);
        } else {
#pragma unroll
            for (int i = 0; i < 4; ++i) { *(LAS bf16x8*)(mine + i * 1024) = t[4 + i]; qf[i] = t[i]; }
            lds_post(lds0 + QX_OFF + 4 * g, 1u); AWAIT1(QX_OFF + 4 * (g ^ 1), 1);
#pragma unroll
            for (int i = 0; i < 4; ++i) qf[4 + i] = *(const LAS bf16x8*)(theirs + i * 1024);
        }
    }
    unsigned tok = 1;
    asm volatile("" :: "v"(tokv));
    if (g == 0) {
        while (__any(tokv == tok)) ++tok;
        if (l == 0) __hip_atomic_store((gu32*)(ws + WS_M + wg * 128), tok, RLX_AGENT);
    }
    f32x16 o[4] = {}; f32x16 negm; float m, lsum = 0.f;
    {
        const unsigned kb0 = 1024 * (r >> 2) + 64 * (r & 3) + 16 * ((hh) ^ ((r >> 2) & 3));
        const unsigned kb1 = 1024 * (r >> 2) + 64 * (r & 3) + 16 * ((2 + hh) ^ ((r >> 2) & 3));
        const int blk = (l >> 4) & 1, q = (l & 15) >> 2, p = l & 3;
        const unsigned vb0 = 16384 + 1024 * hh + 64 * q + 16 * ((2 * blk + (p >> 1)) ^ (hh)) + 8 * (p & 1);
        const unsigned vb1 = 16384 + 2048 + 1024 * hh + 64 * q + 16 * ((2 * blk + (p >> 1)) ^ (2 + hh)) + 8 * (p & 1);
        u32x4 pA0, pA1, pB0 = {}, pB1 = {}; bf16x8 kf[8], vf0[4] = {}, vf1[4] = {};
        f32x16 sA = {}, sB;
        {
#pragma unroll
            for (int ks = 0; ks < 8; ++ks) sA = MFMA(*(const LAS bf16x8*)(lds + K0_OFF + ((ks & 1) ? kb1 : kb0) + 256 * (ks >> 1)), qf[ks], sA);
            m = swap32max(max16(sA));
#pragma unroll
            for (int i = 0; i < 16; ++i) { sA[i] -= m; negm[i] = -m; }
        }
#define KRD(ks) kf[ks] = *(const LAS bf16x8*)(sb + (((ks) & 1) ? kb1 : kb0) + 256 * ((ks) >> 1))
#define VRD(V, off) V = cat(trd(sb + vb0 + (off)), trd(sb + vb1 + (off)))
#define SMX(C, P0, P1, i) do { e_[(i) & 3] = EX(C[i]); if ((i) > 0) ps += e_[((i) - 1) & 3]; \
            if (((i) & 1) == 0 && (i) > 0) { const unsigned w_ = cvtpk(e_[((i) - 2) & 3], e_[((i) - 1) & 3]); if ((i) <= 8) { P0[(((i) - 2) >> 1) & 3] = w_; PIN(P0); } else { P1[(((i) - 2) >> 1) & 3] = w_; PIN(P1); } } \
            PIN(e_[(i) & 3]); PIN(ps); } while (0)
#define FIXUP(C, N, P0, P1) do { if (__builtin_expect(__any(!(ps <= PSUM_MAX)), 0)) { \
            asm volatile("s_nop 15" : "+v"(o[0]), "+v"(o[1]), "+v"(o[2]), "+v"(o[3]), "+v"(N)); \
            const float delta_ = fmaxf(swap32max(max16(C)), 0.f), f_ = EX(-delta_); m += delta_; lsum *= f_; ps = 0.f; \
            _Pragma("unroll") for (int i = 0; i < 16; ++i) { C[i] = EX(C[i] - delta_); ps += C[i]; N[i] -= delta_; negm[i] = -m; } \
            P0 = __builtin_bit_cast(u32x4, pack8(C, 0)); P1 = __builtin_bit_cast(u32x4, pack8(C, 8)); \
            _Pragma("unroll") for (int db = 0; db < 4; ++db) _Pragma("unroll") for (int i = 0; i < 16; ++i) o[db][i] *= f_; \
            asm volatile("s_nop 1" : "+v"(o[0]), "+v"(o[1]), "+v"(o[2]), "+v"(o[3]), "+v"(negm), "+v"(P0), "+v"(P1)); } } while (0)
#define STEP(C, N, P0, P1, Q0, Q1, MID, LATE) do { float ps = 0.f, e_[4]; \
            AMM(o[0], vf0[0], Q0); SB(); KRD(0); KRD(1); SB(); \
            AMM(o[1], vf0[1], Q0); SB(); KRD(2); KRD(3); SB(); \
            AMM(o[2], vf0[2], Q0); SB(); KRD(4); KRD(5); asm volatile("" : "+v"(C)); SMX(C, P0, P1, 0); SB(); \
            AMM(o[3], vf0[3], Q0); SB(); KRD(6); KRD(7); SMX(C, P0, P1, 1); SB(); \
            MID; \
            AMM(o[0], vf1[0], Q1); SB(); VRD(vf0[0], 0); SMX(C, P0, P1, 2); SB(); \
            AMM(o[1], vf1[1], Q1); SB(); VRD(vf0[1], 256); SMX(C, P0, P1, 3); SB(); \
            AMM(o[2], vf1[2], Q1); SB(); VRD(vf0[2], 512); SMX(C, P0, P1, 4); SB(); \
            AMM(o[3], vf1[3], Q1); SB(); VRD(vf0[3], 768); SMX(C, P0, P1, 5); SB(); \
            AMM0(N, kf[0], qf[0], negm); SB(); VRD(vf1[0], 4096); SMX(C, P0, P1, 6); SB(); \
            AMM(N, kf[1], qf[1]); SB(); VRD(vf1[1], 4096 + 256); SMX(C, P0, P1, 7); SB(); \
            AMM(N, kf[2], qf[2]); SB(); VRD(vf1[2], 4096 + 512); SMX(C, P0, P1, 8); SB(); \
            AMM(N, kf[3], qf[3]); SB(); VRD(vf1[3], 4096 + 768); SMX(C, P0, P1, 9); SB(); \
            AMM(N, kf[4], qf[4]); SB(); LATE; SMX(C, P0, P1, 10); SMX(C, P0, P1, 11); SB(); \
            AMM(N, kf[5], qf[5]); SB(); SMX(C, P0, P1, 12); SMX(C, P0, P1, 13); SB(); \
            AMM(N, kf[6], qf[6]); SB(); SMX(C, P0, P1, 14); SMX(C, P0, P1, 15); SB(); \
            AMM(N, kf[7], qf[7]); SB(); ps += e_[3]; P1[3] = cvtpk(e_[2], e_[3]); PIN(P1); PIN(ps); SB(); \
            FIXUP(C, N, P0, P1); \
            lsum += ps; } while (0)
        asm volatile("s_nop 1" : "+v"(o[0]), "+v"(o[1]), "+v"(o[2]), "+v"(o[3]), "+v"(negm), "+v"(pB0), "+v"(pB1), "+v"(vf0[0]), "+v"(vf0[1]), "+v"(vf0[2]), "+v"(vf0[3]),
                     "+v"(vf1[0]), "+v"(vf1[1]), "+v"(vf1[2]), "+v"(vf1[3]));
        __builtin_amdgcn_s_waitcnt(0xC07F);
        for (int u = 0; u < 15; ++u) {
            if (have <= (unsigned)(20 + 2 * u)) AWAITP(have, PL_OFF, 20 + 2 * u, 1);
            asm volatile("" ::: "memory");
            u32x4 nfv;
            { const LAS char* sb = lds + ((u + 2) & 3) * SLOT;        STEP(sA, sB, pA0, pA1, pB0, pB1, if (have <= (unsigned)(21 + 2 * u)) AWAITP(have, PL_OFF, 21 + 2 * u, 1), (void)0); }
            { const LAS char* sb = lds + ((u + 2) & 3) * SLOT + 8192; STEP(sB, sA, pB0, pB1, pA0, pA1, (void)0,
                  { lds_post(pcw, 11 + u); nfv = lds_peek4(lds + PL_OFF); }); }
            have = min4(nfv);
        }
        {
            const LAS char* sb = lds + 1 * SLOT;
            if (have <= 50u) AWAITP(have, PL_OFF, 50, 1);
            { float ps = 0.f, e_[4];
              AMM(o[0], vf0[0], pB0); SB(); KRD(0); KRD(1); SB();
              AMM(o[1], vf0[1], pB0); SB(); KRD(2); KRD(3); SB();
              AMM(o[2], vf0[2], pB0); SB(); KRD(4); KRD(5); asm volatile("" : "+v"(sA)); SMX(sA, pA0, pA1, 0); SB();
              AMM(o[3], vf0[3], pB0); SB(); KRD(6); KRD(7); SMX(sA, pA0, pA1, 1); SB();
              AMM(o[0], vf1[0], pB1); SB(); SMX(sA, pA0, pA1, 2); SB();
              AMM(o[1], vf1[1], pB1); SB(); SMX(sA, pA0, pA1, 3); SB();
              AMM(o[2], vf1[2], pB1); SB(); SMX(sA, pA0, pA1, 4); SB();
              AMM(o[3], vf1[3], pB1); SB(); SMX(sA, pA0, pA1, 5); SB();
              AMM0(sB, kf[0], qf[0], negm); SB(); SMX(sA, pA0, pA1, 6); SB();
              AMM(sB, kf[1], qf[1]); SB(); SMX(sA, pA0, pA1, 7); SB();
              AMM(sB, kf[2], qf[2]); SB(); SMX(sA, pA0, pA1, 8); SB();
              AMM(sB, kf[3], qf[3]); SB(); SMX(sA, pA0, pA1, 9); SB();
              AMM(sB, kf[4], qf[4]); SB(); SMX(sA, pA0, pA1, 10); SMX(sA, pA0, pA1, 11); SB();
              AMM(sB, kf[5], qf[5]); SB(); SMX(sA, pA0, pA1, 12); SMX(sA, pA0, pA1, 13); SB();
              AMM(sB, kf[6], qf[6]); SB(); SMX(sA, pA0, pA1, 14); SMX(sA, pA0, pA1, 15); SB();
              AMM(sB, kf[7], qf[7]); SB(); ps += e_[3]; pA1[3] = cvtpk(e_[2], e_[3]); PIN(pA1); PIN(ps); SB();
              FIXUP(sA, sB, pA0, pA1);
              lsum += ps; }
            asm volatile("s_nop 15" : "+v"(sB));
            { const float delta_ = ceilf(fmaxf(swap32max(max16(sB)) - 8.f, 0.f));
              if (__builtin_expect(__any(delta_ > 0.f), 0)) {
                  const float f_ = EX(-delta_); m += delta_; lsum *= f_;
#pragma unroll
                  for (int i = 0; i < 4; ++i) { pA0[i] = cvtpk(__uint_as_float(pA0[i] << 16) * f_, __uint_as_float(pA0[i] & 0xffff0000u) * f_);
                                                pA1[i] = cvtpk(__uint_as_float(pA1[i] << 16) * f_, __uint_as_float(pA1[i] & 0xffff0000u) * f_); }
#pragma unroll
                  for (int i = 0; i < 16; ++i) sB[i] -= delta_;
#pragma unroll
                  for (int db = 0; db < 4; ++db)
#pragma unroll
                      for (int i = 0; i < 16; ++i) o[db][i] *= f_;
              }
              float ps = 0.f;
#pragma unroll
              for (int i = 0; i < 16; ++i) { sB[i] = EX(sB[i]); ps += sB[i]; }
              lsum += ps;
              pB0 = __builtin_bit_cast(u32x4, pack8(sB, 0)); pB1 = __builtin_bit_cast(u32x4, pack8(sB, 8));
              asm volatile("s_nop 1" : "+v"(o[0]), "+v"(o[1]), "+v"(o[2]), "+v"(o[3]), "+v"(pA0), "+v"(pA1), "+v"(pB0), "+v"(pB1), "+v"(lsum)); }
            if (have <= 51u) AWAITP(have, PL_OFF, 51, 1);
            bf16x8 vg0[4], vg1[4];
            VRD(vf0[0], 0); VRD(vf0[1], 256); VRD(vf0[2], 512); VRD(vf0[3], 768);
            VRD(vf1[0], 4096); VRD(vf1[1], 4096 + 256); VRD(vf1[2], 4096 + 512); VRD(vf1[3], 4096 + 768); SB();
            AMM(o[0], vf0[0], pA0); VRD(vg0[0], 8192); SB(); AMM(o[1], vf0[1], pA0); VRD(vg0[1], 8192 + 256); SB();
            AMM(o[2], vf0[2], pA0); VRD(vg0[2], 8192 + 512); SB(); AMM(o[3], vf0[3], pA0); VRD(vg0[3], 8192 + 768); SB();
            AMM(o[0], vf1[0], pA1); VRD(vg1[0], 12288); SB(); AMM(o[1], vf1[1], pA1); VRD(vg1[1], 12288 + 256); SB();
            AMM(o[2], vf1[2], pA1); VRD(vg1[2], 12288 + 512); SB(); AMM(o[3], vf1[3], pA1); VRD(vg1[3], 12288 + 768); SB();
            AMM(o[0], vg0[0], pB0); AMM(o[1], vg0[1], pB0); AMM(o[2], vg0[2], pB0); AMM(o[3], vg0[3], pB0);
            AMM(o[0], vg1[0], pB1); AMM(o[1], vg1[1], pB1); AMM(o[2], vg1[2], pB1); AMM(o[3], vg1[3], pB1);
            asm volatile("s_nop 15" : "+v"(o[0]), "+v"(o[1]), "+v"(o[2]), "+v"(o[3]));
        }
    }
    {
        if (g == 0) { VMW(0); lds_post(lds0 + TOK_OFF, tok); }
        lds_post(pcw, 26);
        const float inv = 1.0f / swap32sum(lsum);
        bf16x8 of[8];
#pragma unroll
        for (int db = 0; db < 4; ++db) {
#pragma unroll
            for (int i = 0; i < 16; ++i) o[db][i] *= inv;
            of[2 * db] = pack8(o[db], 0); of[2 * db + 1] = pack8(o[db], 8);
        }
#pragma unroll
        for (int s = 0; s < 8; ++s) __builtin_amdgcn_raw_buffer_store_b128(__builtin_bit_cast(u32x4, of[s]), rWS, l * 16, WS_O + ((wg * 4 + g) * 8 + s) * 1024, 16);
        const int blk = (l >> 4) & 1, q = (l & 15) >> 2, p = l & 3;
        const unsigned ob = 1280 * hh + 64 * q + 16 * (2 * blk + (p >> 1)) + 8 * (p & 1);
        f32x16 acc[5] = {};
        bf16x8 aA[5], aB[5];
        asm volatile("s_nop 1" : "+v"(acc[0]), "+v"(acc[1]), "+v"(acc[2]), "+v"(acc[3]), "+v"(acc[4]), "+v"(of[0]), "+v"(of[1]), "+v"(of[2]), "+v"(of[3]), "+v"(of[4]), "+v"(of[5]), "+v"(of[6]), "+v"(of[7]));
#define ORD(A, s, nb) A[nb] = cat(trd(wb + 5120 * (s) + 256 * (nb)), trd(wb + 5120 * (s) + 2560 + 256 * (nb)))
#define OSTEP(AO, AN, F, sn) do { \
            AMM(acc[0], AO[0], F); if ((sn) < 8) ORD(AN, (sn) & 7, 0); SB(); AMM(acc[1], AO[1], F); if ((sn) < 8) ORD(AN, (sn) & 7, 1); SB(); AMM(acc[2], AO[2], F); if ((sn) < 8) ORD(AN, (sn) & 7, 2); SB(); \
            AMM(acc[3], AO[3], F); if ((sn) < 8) ORD(AN, (sn) & 7, 3); SB(); AMM(acc[4], AO[4], F); if ((sn) < 8) ORD(AN, (sn) & 7, 4); SB(); } while (0)
#define OHEAD(F, off) do { const LAS char* wb = lds + (off) + ob; ORD(aA, 0, 0); ORD(aA, 0, 1); ORD(aA, 0, 2); ORD(aA, 0, 3); ORD(aA, 0, 4); SB(); \
            OSTEP(aA, aB, F[0], 1); OSTEP(aB, aA, F[1], 2); OSTEP(aA, aB, F[2], 3); OSTEP(aB, aA, F[3], 4); OSTEP(aA, aB, F[4], 5); OSTEP(aB, aA, F[5], 6); OSTEP(aA, aB, F[6], 7); OSTEP(aB, aA, F[7], 8); } while (0)
        bf16x8 f1[8] = {}, f2[8] = {};
#define f3 of
#define LDS1(R, SO) asm volatile("buffer_load_dwordx4 %0, %1, %2, %3 offen sc1" : "+v"(R) : "v"(l16), "s"(rWS), "s"(SO) : "memory")
#define OLOAD(F, cc) do { const unsigned so_ = WS_O + ((((((b << 2) | ((hl + (cc)) & 3)) << 3) | xg) * 4 + g) * 8) * 1024; _Pragma("unroll") for (int s = 0; s < 8; ++s) LDS1(F[s], so_ + s * 1024); } while (0)
#define FPIN(F) asm volatile("s_waitcnt vmcnt(0)" : "+v"(F[0]), "+v"(F[1]), "+v"(F[2]), "+v"(F[3]), "+v"(F[4]), "+v"(F[5]), "+v"(F[6]), "+v"(F[7]) :: "memory")
        const unsigned l16 = l * 16;
        unsigned taken = 0;
#define RDY() ({ const unsigned m_ = lds_peek(lds + SY_OFF) & 15u; asm volatile("" ::: "memory"); (((m_ | (m_ << 4)) >> hl) & 15u) & ~taken; })
#define TRY(MASK) do { const unsigned r_ = RDY() & (MASK); if (((MASK) & 2u) && (r_ & 2u)) OLOAD(f1, 1); if (((MASK) & 4u) && (r_ & 4u)) OLOAD(f2, 2); if (((MASK) & 8u) && (r_ & 8u)) OLOAD(f3, 3); taken |= r_; } while (0)
#define NEED(C, MASK) do { if ((taken & (MASK)) != (MASK)) TRY(MASK); for (int sp_ = 0; !(taken & (1u << (C))) && sp_ < SPIN_CAP; ++sp_) { __builtin_amdgcn_s_sleep(1); TRY(MASK); } } while (0)
        AWAITP(have, PL_OFF, 52, 1);
        { const LAS char* wb = lds + 65536 + ob; ORD(aA, 0, 0); ORD(aA, 0, 1); ORD(aA, 0, 2); ORD(aA, 0, 3); ORD(aA, 0, 4); SB();
          OSTEP(aA, aB, of[0], 1); OSTEP(aB, aA, of[1], 2); OSTEP(aA, aB, of[2], 3); OSTEP(aB, aA, of[3], 4);
          VMW(0); lds_post(pcw, 27);
          TRY(6u);
          OSTEP(aA, aB, of[4], 5); OSTEP(aB, aA, of[5], 6); OSTEP(aA, aB, of[6], 7); OSTEP(aB, aA, of[7], 8); }
        lds_post(pcw, 28);
        if (have <= 54u) AWAITP(have, PL_OFF, 54, 1);
        NEED(1, 14u); FPIN(f1); OHEAD(f1, 106496);
        NEED(2, 12u); FPIN(f2); OHEAD(f2, 0);
        if (have <= 55u) AWAITP(have, PL_OFF, 55, 1);
        NEED(3, 8u); FPIN(f3); OHEAD(f3, 65536);
        asm volatile("s_nop 15" : "+v"(acc[0]), "+v"(acc[1]), "+v"(acc[2]), "+v"(acc[3]), "+v"(acc[4]));
#pragma unroll
        for (int u = 0; u < 10; ++u) __builtin_amdgcn_raw_buffer_store_b128(__builtin_bit_cast(u32x4, pack8(acc[u >> 1], 8 * (u & 1))), rWS, l * 16, WS_P + (wg * 40 + g * 10 + u) * 1024, 16);
        VMW(0);
        lds_post(pcw, 29);
        AWAIT1(SY_OFF, 31);
        REDUCE(g);
    }
}

extern "C" void kernel_launch(void* const* d_in, const int* in_sizes, int n_in, void* d_out, int out_size, void* d_ws, size_t ws_size, hipStream_t stream) {
    (void)hipFuncSetAttribute((const void*)k_fused, hipFuncAttributeMaxDynamicSharedMemorySize, LDS_BYTES);
    k_fused<<<256, 512, LDS_BYTES, stream>>>((const char*)d_in[0], (const char*)d_in[1], (const char*)d_in[2], (const char*)d_in[3], (const char*)d_in[4], (char*)d_out, (char*)d_ws);
}
```
